```python
import torch

def _softmax(x, axis=-1):
    return torch.softmax(x, dim=axis)

def _gelu(x):
    return torch.nn.functional.gelu(x, approximate='tanh')

def _silu(x):
    return torch.nn.functional.silu(x)

def _sigmoid(x):
    return torch.sigmoid(x)

def _layernorm(x, axis=-1, eps=1e-05):
    m = x.mean(dim=axis, keepdim=True)
    v = x.var(dim=axis, keepdim=True, unbiased=False)
    return (x - m) / torch.sqrt(v + eps)

def _scatter_add(tgt, idx, src, axis=0):
    return tgt.clone().index_add_(axis, idx, src)

def _segment_sum(src, seg, n):
    out = torch.zeros((n,) + src.shape[1:], dtype=src.dtype)
    return out.index_add_(0, seg, src)

def _pad(x, pw):
    flat = tuple((v for pair in reversed(pw) for v in pair))
    return torch.nn.functional.pad(x, flat)
kernel_name = 'gated_mix__memory_aug_recurrent_mem_tr__moe_arch_mod__prompt'

def setup_inputs():

    _w = lambda a: (lambda t: t.to(torch.bfloat16) if t.is_floating_point() else t)(torch.as_tensor(a))

    def _A_setup():
        B, S, Dm = (1, 2048, 448)
        H, Dh = (16, 256)
        M, seg = (8, 32)
        seg = min(seg, S)
        S = S // seg * seg
        return {'x': _w((lambda *s: torch.randn(*s, dtype=torch.bfloat16) * 0.05)(B, S, Dm)), 'mem0': _w((lambda *s: torch.randn(*s, dtype=torch.bfloat16) * 0.8)(B, M, Dm)), 'Wq': _w((lambda *s: torch.randn(*s, dtype=torch.bfloat16) * 1.6)(Dm, H, Dh) * Dm ** (-0.5)), 'Wk': _w((lambda *s: torch.randn(*s, dtype=torch.bfloat16) * 1.6)(Dm, H, Dh) * Dm ** (-0.5)), 'Wv': _w((lambda *s: torch.randn(*s, dtype=torch.bfloat16) * 0.8)(Dm, H, Dh) * Dm ** (-0.5)), 'Wo': _w((lambda *s: torch.randn(*s, dtype=torch.bfloat16) * 0.8)(H, Dh, Dm) * (H * Dh) ** (-0.5)), 'seg_len': seg}
    _A_in = _A_setup()

    def _B_setup():
        B, S, D, F, C = (1, 2048, 448, 1792, 1)
        return {'x': _w((lambda *s: torch.randn(*s, dtype=torch.bfloat16) * 0.05)(B, S, D)), 'w_r': _w((lambda *s: torch.randn(*s, dtype=torch.bfloat16) * 1.6)(D)), 'w_gate': _w((lambda *s: torch.randn(*s, dtype=torch.bfloat16) * 0.2)(D, F)), 'w_up': _w((lambda *s: torch.randn(*s, dtype=torch.bfloat16) * 0.2)(D, F)), 'w_down': _w((lambda *s: torch.randn(*s, dtype=torch.bfloat16) * 0.2)(F, D))}
    _B_in = _B_setup()
    return {'A_x': _A_in['x'], 'A_mem0': _A_in['mem0'], 'A_Wq': _A_in['Wq'], 'A_Wk': _A_in['Wk'], 'A_Wv': _A_in['Wv'], 'A_Wo': _A_in['Wo'], 'A_seg_len': _A_in['seg_len'], 'B_x': _B_in['x'], 'B_w_r': _B_in['w_r'], 'B_w_gate': _B_in['w_gate'], 'B_w_up': _B_in['w_up'], 'B_w_down': _B_in['w_down'], 'gate_w': _w((lambda *s: torch.randn(*s, dtype=torch.bfloat16) * 0.05)(448) * 16.0)}

def reference(A_x, A_mem0, A_Wq, A_Wk, A_Wv, A_Wo, A_seg_len, B_x, B_w_r, B_w_gate, B_w_up, B_w_down, gate_w):

    def _A_ref(x, mem0, Wq, Wk, Wv, Wo, seg_len):
        B, S, Dm = x.shape
        H, Dh = (Wq.shape[1], Wq.shape[2])
        M = mem0.shape[1]
        scale = Dh ** (-0.5)
        n_seg = S // seg_len
        L = M + seg_len
        idx = torch.arange(L)
        mask = (idx[:, None] < M) | (idx[:, None] >= idx[None, :])
        mem = mem0
        outs = []
        for s in range(n_seg):
            xs = x[:, s * seg_len:(s + 1) * seg_len, :].clone()
            h = torch.concatenate([mem, xs], axis=1)
            q = torch.einsum('btd,dhe->bhte', h, Wq)
            k = torch.einsum('btd,dhe->bhte', h, Wk)
            v = torch.einsum('btd,dhe->bhte', h, Wv)
            scores = torch.einsum('bhqe,bhke->bhqk', q, k) * scale
            scores = torch.where(mask[None, None], scores, -1000000000.0)
            p = _softmax(scores, axis=-1)
            o = torch.einsum('bhqk,bhke->bhqe', p, v)
            o = torch.einsum('bhte,hed->btd', o, Wo)
            mem = o[:, :M, :].clone()
            outs.append(o[:, M:, :])
        return torch.concatenate(outs, axis=1)

    def _B_ref(x, w_r, w_gate, w_up, w_down):
        B, S, D = x.shape
        C = 1
        r = x @ w_r
        idx = torch.argsort(-r, axis=-1)[:, :C]
        idx_e = (lambda x, n, axis: torch.repeat_interleave(x, n, dim=axis))(idx.reshape(B, C, 1), D, axis=2)
        x_sel = (lambda x, i, axis: torch.take_along_dim(x, i, dim=axis))(x, idx_e, axis=1)
        g = _silu(x_sel @ w_gate)
        u = x_sel @ w_up
        y = g * u @ w_down
        r_sel = _sigmoid((lambda x, i, axis: torch.take_along_dim(x, i, dim=axis))(r, idx, axis=1))
        y = y * r_sel.reshape(B, C, 1)
        b_off = (torch.arange(B) * S).reshape(B, 1)
        flat_idx = (idx + b_off).reshape(B * C)
        zero = (x * 0.0).reshape(B * S, D)
        upd = _scatter_add(zero, flat_idx, y.reshape(B * C, D), axis=0)
        return x + upd.reshape(B, S, D)
    x = A_x
    g = _sigmoid((x * gate_w).sum(-1, keepdims=True))
    a = _A_ref(x, A_mem0, A_Wq, A_Wk, A_Wv, A_Wo, A_seg_len)
    b = _B_ref(x, B_w_r, B_w_gate, B_w_up, B_w_down)
    return g * a + (1 - g) * b
```

```cpp
#include <hip/hip_runtime.h>
#include <hip/hip_bf16.h>
#include <stdint.h>

typedef __bf16 bf16x2 __attribute__((ext_vector_type(2)));
typedef __bf16 bf16x8 __attribute__((ext_vector_type(8)));
typedef __bf16 bf16x16 __attribute__((ext_vector_type(16)));
typedef short s16x4 __attribute__((ext_vector_type(4)));
typedef float f32x2 __attribute__((ext_vector_type(2)));
typedef float f32x4 __attribute__((ext_vector_type(4)));
typedef unsigned u32x2 __attribute__((ext_vector_type(2)));
typedef unsigned u32x4 __attribute__((ext_vector_type(4)));
typedef unsigned u32x8 __attribute__((ext_vector_type(8)));
typedef unsigned short u16;
#define LAS __attribute__((address_space(3)))
#define GAS __attribute__((address_space(1)))

constexpr int DM = 448, NH = 16, NSEG = 64, SEQ = 2048, FF = 1792;
constexpr int V_IMM = 4;
constexpr int NTA = 4, NTB = 2, NTO = 4;
#define XV 2
constexpr unsigned SENT = 0xFFFFFFFFu;
constexpr int AUX_SC1 = 16;
constexpr float SM_C = 1.4426950408889634f / 16.f;

constexpr unsigned TILE_B = 7 * 2048;
constexpr unsigned OFF_WA = 0;
constexpr unsigned OFF_WB = OFF_WA + 16 * 4 * 7 * TILE_B;
constexpr unsigned OFF_G = OFF_WB + 14 * 4 * 8 * TILE_B;
constexpr unsigned OFF_R = OFF_G + SEQ * 4;
constexpr unsigned OFF_LAT = OFF_R + SEQ * 4;
constexpr unsigned OFF_XK = 13u << 20;
constexpr unsigned OFF_SENT = 15u << 20;
constexpr unsigned MEM_STRIDE = 7168;
constexpr unsigned PM_SLOT = 32;
constexpr unsigned RING_WIN = 256u << 10;
constexpr unsigned OFF_RING = OFF_SENT;
constexpr unsigned OFF_CFG = OFF_RING + 2 * RING_WIN;
constexpr unsigned OFF_FIN = OFF_CFG + 512;
constexpr unsigned OFF_RDY = OFF_CFG + 640;
constexpr int PT_ARM = NSEG - 2;
constexpr unsigned OFF_PTF = OFF_RDY + 16 * 64;
constexpr unsigned OFF_WF = OFF_PTF + NSEG * 16 * 64;
constexpr unsigned OFF_WTA = OFF_WF + NSEG * 28 * 64;
constexpr unsigned OFF_YP = OFF_WTA + 2 * 28 * 4096;
constexpr unsigned OFF_VF = OFF_YP + 28 * DM * 4;
constexpr unsigned OFF_MEMT1 = OFF_VF + NSEG * 28 * 64;
constexpr unsigned OFF_PTA = OFF_MEMT1 + (NSEG - 1) * MEM_STRIDE;
constexpr unsigned OFF_SENT_END = OFF_PTA + (NSEG - PT_ARM) * 16 * 2560;
constexpr unsigned OFF_PT = OFF_SENT_END;
constexpr unsigned OFF_MEM0 = OFF_PT + PT_ARM * 16 * 2560;
constexpr unsigned OFF_PROF = 19u << 20;
constexpr unsigned OFF_VB = 20u << 20;
constexpr unsigned OFF_WT = OFF_VB + NSEG * 14 * 32768;
constexpr unsigned WS_USED = OFF_WT + NSEG * 28 * 4096;
static_assert(OFF_LAT + 1024 <= OFF_XK, "layout");
static_assert(OFF_MEM0 + MEM_STRIDE <= OFF_PROF, "layout");
static_assert((OFF_SENT_END - OFF_SENT) % 16 == 0 && OFF_MEMT1 % 128 == 0, "layout");
__device__ __forceinline__ unsigned ring_off(const char* ws) { return OFF_RING + ((0u - (unsigned)(uintptr_t)ws - OFF_RING) & (RING_WIN - 1)); }
__device__ __forceinline__ unsigned pt_off(int s) { return s < PT_ARM ? OFF_PT + s * 16 * 2560 : OFF_PTA + (s - PT_ARM) * 16 * 2560; }
__device__ __forceinline__ unsigned memt_off(int s) { return s == 0 ? OFF_MEM0 : OFF_MEMT1 + (s - 1) * MEM_STRIDE; }

#define SL_TOK 2
#define GRID_PI 3000
#define GRID_PHI 2100
#define K1_AUX 16
#define K1_LD 0
#define GRID_T0 2300
#define STAG_A 6
#define STAG_B 0
#define TOUCH_AT 260
#define TOUCH_AHEAD 2
#define DERATE 4240
#define CTL_MAX 30
#define CTL_DPI 28
#define CTL_FIRST 5
#define CTL_RDEC 4
#define CTL_DEAD 6
#define CTL_WIN 1000
#define FAB_PI 600
#define FAB_PHI 300
#define P_NEAR_S 0
#define FIN_A 10
#define PREPOLL 1
constexpr int K2_LDS = 160 * 1024;
#define POLL_BUDGET (1 << 16)

__device__ __forceinline__ float bf2f(unsigned lo16) { return __uint_as_float(lo16 << 16); }
__device__ __forceinline__ float bflo(unsigned d) { return __uint_as_float(d << 16); }
__device__ __forceinline__ float bfhi(unsigned d) { return __uint_as_float(d & 0xffff0000u); }
__device__ __forceinline__ unsigned pk(float lo, float hi) {
  f32x2 v = {lo, hi};
  return __builtin_bit_cast(unsigned, __builtin_convertvector(v, bf16x2));
}
__device__ __forceinline__ float rbf(float v) { return bflo(pk(v, 0.f) & 0xffffu); }
__device__ __forceinline__ unsigned canon(unsigned d) { return d < 0xFFFFFFFEu ? d : 0xFFFFFFFEu; }
template <int CTRL> __device__ __forceinline__ float dpp(float v) {
  return __builtin_bit_cast(float, __builtin_amdgcn_mov_dpp(__builtin_bit_cast(int, v), CTRL, 0xf, 0xf, true));
}
__device__ __forceinline__ float sigmoidf_(float v) { return 1.f / (1.f + __expf(-v)); }
__device__ __forceinline__ bf16x8 asb8(u32x4 v) { return __builtin_bit_cast(bf16x8, v); }
__device__ __forceinline__ bf16x16 cat16(u32x4 a, u32x4 b) {
  u32x8 v = {a[0], a[1], a[2], a[3], b[0], b[1], b[2], b[3]};
  return __builtin_bit_cast(bf16x16, v);
}
__device__ __forceinline__ bf16x8 half8(const bf16x16& v, int z) {
  return z == 0 ? __builtin_shufflevector(v, v, 0, 1, 2, 3, 4, 5, 6, 7)
                : __builtin_shufflevector(v, v, 8, 9, 10, 11, 12, 13, 14, 15);
}
__device__ __forceinline__ f32x4 mf32(bf16x8 a, bf16x8 b, f32x4 c) {
  return __builtin_amdgcn_mfma_f32_16x16x32_bf16(a, b, c, 0, 0, 0);
}
__device__ __forceinline__ f32x4 mf16(u32x2 a, u32x2 b, f32x4 c) {
  return mf32(asb8((u32x4){a[0], a[1], 0u, 0u}), asb8((u32x4){b[0], b[1], 0u, 0u}), c);
}
__device__ __forceinline__ u32x2 ldtr(const char* p) {
  return __builtin_bit_cast(u32x2, __builtin_amdgcn_ds_read_tr16_b64_v4i16((LAS s16x4*)p));
}
__device__ __forceinline__ unsigned umax3(unsigned a, unsigned b, unsigned c) { return max(max(a, b), c); }
__device__ __forceinline__ unsigned umax4(unsigned m, u32x4 v) { return umax3(umax3(m, v[0], v[1]), v[2], v[3]); }
__device__ __forceinline__ void wg_barrier() {
  asm volatile("s_waitcnt lgkmcnt(0)\n\ts_barrier" ::: "memory");
}

__device__ __forceinline__ bf16x8 trpair(const char* p) {
  const u32x2 lo = ldtr(p), hi = ldtr(p + 16 * 224);
  return asb8((u32x4){lo[0], lo[1], hi[0], hi[1]});
}
#define K1_POL ""
__device__ __forceinline__ void glds16(const void* g, unsigned lds) {
  unsigned keep;
  asm volatile("s_mov_b32 %0, m0\n\ts_mov_b32 m0, %2\n\ts_nop 0\n\tglobal_load_lds_dwordx4 %1, off" K1_POL "\n\ts_mov_b32 m0, %0"
               : "=&s"(keep) : "v"(g), "s"(lds) : "memory");
}
__device__ __forceinline__ void gld16(u32x4& d, const void* g) {
  asm volatile("global_load_dwordx4 %0, %1, off" : "=v"(d) : "v"(g) : "memory");
}

typedef __amdgpu_buffer_rsrc_t rsrc_t;
__device__ __forceinline__ u32x4 ld16(rsrc_t r, unsigned v, unsigned s) { return __builtin_amdgcn_raw_buffer_load_b128(r, v, s, AUX_SC1); }
__device__ __forceinline__ u32x2 ld8(rsrc_t r, unsigned v, unsigned s) { return __builtin_amdgcn_raw_buffer_load_b64(r, v, s, AUX_SC1); }
__device__ __forceinline__ unsigned ld4(rsrc_t r, unsigned v, unsigned s) { return __builtin_amdgcn_raw_buffer_load_b32(r, v, s, AUX_SC1); }
__device__ __forceinline__ void st16(rsrc_t r, unsigned v, unsigned s, u32x4 d) { __builtin_amdgcn_raw_buffer_store_b128(d, r, v + s, 0, AUX_SC1); }
__device__ __forceinline__ void st8(rsrc_t r, unsigned v, unsigned s, u32x2 d) { __builtin_amdgcn_raw_buffer_store_b64(d, r, v, s, AUX_SC1); }
__device__ __forceinline__ void st4(rsrc_t r, unsigned v, unsigned s, unsigned d) { __builtin_amdgcn_raw_buffer_store_b32(d, r, v, s, AUX_SC1); }

#define SCORE_ASM \
  "s_mov_b32 s40, %16\n" \
  "s_add_u32 s44, %12, 0\n" \
  "s_sub_u32 s45, s44, 7\n" \
  "s_cmp_lt_u32 s44, 7\n" \
  "s_cselect_b32 s44, s44, s45\n" \
  "s_lshl_b32 s90, s44, 10\n" \
  "s_lshl_b32 s44, s44, 11\n" \
  "s_add_u32 s50, s44, %11\n" \
  "s_add_u32 s44, %12, 1\n" \
  "s_sub_u32 s45, s44, 7\n" \
  "s_cmp_lt_u32 s44, 7\n" \
  "s_cselect_b32 s44, s44, s45\n" \
  "s_lshl_b32 s91, s44, 10\n" \
  "s_lshl_b32 s44, s44, 11\n" \
  "s_add_u32 s51, s44, %11\n" \
  "s_add_u32 s44, %12, 2\n" \
  "s_sub_u32 s45, s44, 7\n" \
  "s_cmp_lt_u32 s44, 7\n" \
  "s_cselect_b32 s44, s44, s45\n" \
  "s_lshl_b32 s92, s44, 10\n" \
  "s_lshl_b32 s44, s44, 11\n" \
  "s_add_u32 s52, s44, %11\n" \
  "s_add_u32 s44, %12, 3\n" \
  "s_sub_u32 s45, s44, 7\n" \
  "s_cmp_lt_u32 s44, 7\n" \
  "s_cselect_b32 s44, s44, s45\n" \
  "s_lshl_b32 s93, s44, 10\n" \
  "s_lshl_b32 s44, s44, 11\n" \
  "s_add_u32 s53, s44, %11\n" \
  "s_add_u32 s44, %12, 4\n" \
  "s_sub_u32 s45, s44, 7\n" \
  "s_cmp_lt_u32 s44, 7\n" \
  "s_cselect_b32 s44, s44, s45\n" \
  "s_lshl_b32 s94, s44, 10\n" \
  "s_lshl_b32 s44, s44, 11\n" \
  "s_add_u32 s54, s44, %11\n" \
  "s_add_u32 s44, %12, 5\n" \
  "s_sub_u32 s45, s44, 7\n" \
  "s_cmp_lt_u32 s44, 7\n" \
  "s_cselect_b32 s44, s44, s45\n" \
  "s_lshl_b32 s95, s44, 10\n" \
  "s_lshl_b32 s44, s44, 11\n" \
  "s_add_u32 s55, s44, %11\n" \
  "s_add_u32 s44, %12, 6\n" \
  "s_sub_u32 s45, s44, 7\n" \
  "s_cmp_lt_u32 s44, 7\n" \
  "s_cselect_b32 s44, s44, s45\n" \
  "s_lshl_b32 s96, s44, 10\n" \
  "s_lshl_b32 s44, s44, 11\n" \
  "s_add_u32 s56, s44, %11\n" \
  "s_mov_b32 s46, %14\n" \
  "s_add_u32 s44, s50, 0\n" \
  "buffer_load_dwordx4 a[0:3], %3, %10, s44 offen nt\n" \
  "buffer_load_dwordx4 a[4:7], %3, %10, s44 offen offset:1024 nt\n" \
  "s_add_u32 s44, s51, 0\n" \
  "buffer_load_dwordx4 a[8:11], %3, %10, s44 offen nt\n" \
  "buffer_load_dwordx4 a[12:15], %3, %10, s44 offen offset:1024 nt\n" \
  "s_add_u32 s44, s52, 0\n" \
  "buffer_load_dwordx4 a[16:19], %3, %10, s44 offen nt\n" \
  "buffer_load_dwordx4 a[20:23], %3, %10, s44 offen offset:1024 nt\n" \
  "s_add_u32 s44, s53, 0\n" \
  "buffer_load_dwordx4 a[24:27], %3, %10, s44 offen nt\n" \
  "buffer_load_dwordx4 a[28:31], %3, %10, s44 offen offset:1024 nt\n" \
  "s_add_u32 s44, s54, 0\n" \
  "buffer_load_dwordx4 a[32:35], %3, %10, s44 offen nt\n" \
  "buffer_load_dwordx4 a[36:39], %3, %10, s44 offen offset:1024 nt\n" \
  "s_add_u32 s44, s55, 0\n" \
  "buffer_load_dwordx4 a[40:43], %3, %10, s44 offen nt\n" \
  "buffer_load_dwordx4 a[44:47], %3, %10, s44 offen offset:1024 nt\n" \
  "s_add_u32 s44, s56, 0\n" \
  "buffer_load_dwordx4 a[48:51], %3, %10, s44 offen nt\n" \
  "buffer_load_dwordx4 a[52:55], %3, %10, s44 offen offset:1024 nt\n" \
  "s_add_u32 s44, s50, 14336\n" \
  "buffer_load_dwordx4 a[56:59], %3, %10, s44 offen nt\n" \
  "buffer_load_dwordx4 a[60:63], %3, %10, s44 offen offset:1024 nt\n" \
  "s_add_u32 s44, s51, 14336\n" \
  "buffer_load_dwordx4 a[64:67], %3, %10, s44 offen nt\n" \
  "buffer_load_dwordx4 a[68:71], %3, %10, s44 offen offset:1024 nt\n" \
  "s_add_u32 s44, s52, 14336\n" \
  "buffer_load_dwordx4 a[72:75], %3, %10, s44 offen nt\n" \
  "buffer_load_dwordx4 a[76:79], %3, %10, s44 offen offset:1024 nt\n" \
  "s_add_u32 s44, s53, 14336\n" \
  "buffer_load_dwordx4 a[80:83], %3, %10, s44 offen nt\n" \
  "buffer_load_dwordx4 a[84:87], %3, %10, s44 offen offset:1024 nt\n" \
  "s_add_u32 s44, s54, 14336\n" \
  "buffer_load_dwordx4 a[88:91], %3, %10, s44 offen nt\n" \
  "buffer_load_dwordx4 a[92:95], %3, %10, s44 offen offset:1024 nt\n" \
  "s_add_u32 s44, s55, 14336\n" \
  "buffer_load_dwordx4 a[96:99], %3, %10, s44 offen nt\n" \
  "buffer_load_dwordx4 a[100:103], %3, %10, s44 offen offset:1024 nt\n" \
  "s_add_u32 s44, s56, 14336\n" \
  "buffer_load_dwordx4 a[104:107], %3, %10, s44 offen nt\n" \
  "buffer_load_dwordx4 a[108:111], %3, %10, s44 offen offset:1024 nt\n" \
  "s_add_u32 s44, s50, 28672\n" \
  "buffer_load_dwordx4 a[112:115], %3, %10, s44 offen nt\n" \
  "buffer_load_dwordx4 a[116:119], %3, %10, s44 offen offset:1024 nt\n" \
  "s_add_u32 s44, s51, 28672\n" \
  "buffer_load_dwordx4 a[120:123], %3, %10, s44 offen nt\n" \
  "buffer_load_dwordx4 a[124:127], %3, %10, s44 offen offset:1024 nt\n" \
  "s_add_u32 s44, s52, 28672\n" \
  "buffer_load_dwordx4 a[128:131], %3, %10, s44 offen nt\n" \
  "buffer_load_dwordx4 a[132:135], %3, %10, s44 offen offset:1024 nt\n" \
  "s_add_u32 s44, s53, 28672\n" \
  "buffer_load_dwordx4 a[136:139], %3, %10, s44 offen nt\n" \
  "buffer_load_dwordx4 a[140:143], %3, %10, s44 offen offset:1024 nt\n" \
  "s_add_u32 s44, s54, 28672\n" \
  "buffer_load_dwordx4 a[144:147], %3, %10, s44 offen nt\n" \
  "buffer_load_dwordx4 a[148:151], %3, %10, s44 offen offset:1024 nt\n" \
  "s_add_u32 s44, s55, 28672\n" \
  "buffer_load_dwordx4 a[152:155], %3, %10, s44 offen nt\n" \
  "buffer_load_dwordx4 a[156:159], %3, %10, s44 offen offset:1024 nt\n" \
  "s_add_u32 s44, s56, 28672\n" \
  "buffer_load_dwordx4 a[160:163], %3, %10, s44 offen nt\n" \
  "buffer_load_dwordx4 a[164:167], %3, %10, s44 offen offset:1024 nt\n" \
  "s_add_u32 s44, s50, 43008\n" \
  "buffer_load_dwordx4 a[168:171], %3, %10, s44 offen nt\n" \
  "buffer_load_dwordx4 a[172:175], %3, %10, s44 offen offset:1024 nt\n" \
  "s_add_u32 s44, s51, 43008\n" \
  "buffer_load_dwordx4 a[176:179], %3, %10, s44 offen nt\n" \
  "buffer_load_dwordx4 a[180:183], %3, %10, s44 offen offset:1024 nt\n" \
  "s_add_u32 s44, s52, 43008\n" \
  "buffer_load_dwordx4 a[184:187], %3, %10, s44 offen nt\n" \
  "buffer_load_dwordx4 a[188:191], %3, %10, s44 offen offset:1024 nt\n" \
  "s_add_u32 s44, s53, 43008\n" \
  "buffer_load_dwordx4 a[192:195], %3, %10, s44 offen nt\n" \
  "buffer_load_dwordx4 a[196:199], %3, %10, s44 offen offset:1024 nt\n" \
  "s_add_u32 s44, s54, 43008\n" \
  "buffer_load_dwordx4 a[200:203], %3, %10, s44 offen nt\n" \
  "buffer_load_dwordx4 a[204:207], %3, %10, s44 offen offset:1024 nt\n" \
  "s_add_u32 s44, s55, 43008\n" \
  "buffer_load_dwordx4 a[208:211], %3, %10, s44 offen nt\n" \
  "buffer_load_dwordx4 a[212:215], %3, %10, s44 offen offset:1024 nt\n" \
  "s_add_u32 s44, s56, 43008\n" \
  "buffer_load_dwordx4 a[216:219], %3, %10, s44 offen nt\n" \
  "buffer_load_dwordx4 a[220:223], %3, %10, s44 offen offset:1024 nt\n" \
  "buffer_load_dwordx4 v[44:47], %3, %10, s46 offen offset:0\n" \
  "buffer_load_dwordx4 v[48:51], %3, %10, s46 offen offset:1024\n" \
  "buffer_load_dwordx4 v[52:55], %3, %10, s46 offen offset:2048\n" \
  "buffer_load_dwordx4 v[56:59], %3, %10, s46 offen offset:3072\n" \
  "s_add_u32 s46, s46, 4096\n" \
  "buffer_load_dwordx4 v[60:63], %3, %10, s46 offen offset:0\n" \
  "buffer_load_dwordx4 v[64:67], %3, %10, s46 offen offset:1024\n" \
  "buffer_load_dwordx4 v[68:71], %3, %10, s46 offen offset:2048\n" \
  "buffer_load_dwordx4 v[72:75], %3, %10, s46 offen offset:3072\n" \
  "s_add_u32 s46, s46, 28672\n" \
  "s_getpc_b64 s[88:89]\n" \
  "L_pf_%=:\n" \
  "v_lshlrev_b32 v118, 3, %3\n" \
  "v_min_u32 v118, L_end_%=-L_pf_%=, v118\n" \
  "global_load_dword v86, v118, s[88:89]\n" \
  "v_lshrrev_b32 v119, 2, %3\n" \
  "v_min_u32 v119, 56, v119\n" \
  "s_mov_b32 s41, %c18\n" \
  "buffer_load_dword v118, v119, %10, s41 offen sc1\n" \
  "s_add_u32 s44, s50, 57344\n" \
  "buffer_load_dwordx4 a[224:227], %3, %10, s44 offen nt\n" \
  "buffer_load_dwordx4 a[228:231], %3, %10, s44 offen offset:1024 nt\n" \
  "s_add_u32 s44, s51, 57344\n" \
  "buffer_load_dwordx4 a[232:235], %3, %10, s44 offen nt\n" \
  "buffer_load_dwordx4 a[236:239], %3, %10, s44 offen offset:1024 nt\n" \
  "s_add_u32 s44, s52, 57344\n" \
  "buffer_load_dwordx4 a[240:243], %3, %10, s44 offen nt\n" \
  "buffer_load_dwordx4 a[244:247], %3, %10, s44 offen offset:1024 nt\n" \
  "s_add_u32 s44, s53, 57344\n" \
  "buffer_load_dwordx4 a[248:251], %3, %10, s44 offen nt\n" \
  "buffer_load_dwordx4 a[252:255], %3, %10, s44 offen offset:1024 nt\n" \
  "s_add_u32 s44, s54, 57344\n" \
  "buffer_load_dwordx4 v[120:123], %3, %10, s44 offen nt\n" \
  "buffer_load_dwordx4 v[124:127], %3, %10, s44 offen offset:1024 nt\n" \
  "s_add_u32 s44, s55, 57344\n" \
  "buffer_load_dwordx4 v[128:131], %3, %10, s44 offen nt\n" \
  "buffer_load_dwordx4 v[132:135], %3, %10, s44 offen offset:1024 nt\n" \
  "s_add_u32 s44, s56, 57344\n" \
  "buffer_load_dwordx4 v[136:139], %3, %10, s44 offen nt\n" \
  "buffer_load_dwordx4 v[140:143], %3, %10, s44 offen offset:1024 nt\n" \
  "s_add_u32 s44, s50, 86016\n" \
  "buffer_load_dwordx4 v[144:147], %3, %10, s44 offen nt\n" \
  "buffer_load_dwordx4 v[148:151], %3, %10, s44 offen offset:1024 nt\n" \
  "s_add_u32 s44, s51, 86016\n" \
  "buffer_load_dwordx4 v[152:155], %3, %10, s44 offen nt\n" \
  "buffer_load_dwordx4 v[156:159], %3, %10, s44 offen offset:1024 nt\n" \
  "s_add_u32 s44, s52, 86016\n" \
  "buffer_load_dwordx4 v[160:163], %3, %10, s44 offen nt\n" \
  "buffer_load_dwordx4 v[164:167], %3, %10, s44 offen offset:1024 nt\n" \
  "s_add_u32 s44, s53, 86016\n" \
  "buffer_load_dwordx4 v[168:171], %3, %10, s44 offen nt\n" \
  "buffer_load_dwordx4 v[172:175], %3, %10, s44 offen offset:1024 nt\n" \
  "s_add_u32 s44, s54, 86016\n" \
  "buffer_load_dwordx4 v[176:179], %3, %10, s44 offen nt\n" \
  "buffer_load_dwordx4 v[180:183], %3, %10, s44 offen offset:1024 nt\n" \
  "s_add_u32 s44, s55, 86016\n" \
  "buffer_load_dwordx4 v[184:187], %3, %10, s44 offen nt\n" \
  "buffer_load_dwordx4 v[188:191], %3, %10, s44 offen offset:1024 nt\n" \
  "s_add_u32 s44, s56, 86016\n" \
  "buffer_load_dwordx4 v[192:195], %3, %10, s44 offen nt\n" \
  "buffer_load_dwordx4 v[196:199], %3, %10, s44 offen offset:1024 nt\n" \
  "s_add_u32 s44, s50, 71680\n" \
  "buffer_load_dwordx4 v[200:203], %3, %10, s44 offen nt\n" \
  "buffer_load_dwordx4 v[204:207], %3, %10, s44 offen offset:1024 nt\n" \
  "s_add_u32 s44, s51, 71680\n" \
  "buffer_load_dwordx4 v[208:211], %3, %10, s44 offen nt\n" \
  "buffer_load_dwordx4 v[212:215], %3, %10, s44 offen offset:1024 nt\n" \
  "s_add_u32 s44, s52, 71680\n" \
  "buffer_load_dwordx4 v[216:219], %3, %10, s44 offen nt\n" \
  "buffer_load_dwordx4 v[220:223], %3, %10, s44 offen offset:1024 nt\n" \
  "s_add_u32 s44, s53, 71680\n" \
  "buffer_load_dwordx4 v[224:227], %3, %10, s44 offen nt\n" \
  "buffer_load_dwordx4 v[228:231], %3, %10, s44 offen offset:1024 nt\n" \
  "s_add_u32 s44, s54, 71680\n" \
  "buffer_load_dwordx4 v[232:235], %3, %10, s44 offen nt\n" \
  "buffer_load_dwordx4 v[236:239], %3, %10, s44 offen offset:1024 nt\n" \
  "s_add_u32 s44, s55, 71680\n" \
  "buffer_load_dwordx4 v[240:243], %3, %10, s44 offen nt\n" \
  "buffer_load_dwordx4 v[244:247], %3, %10, s44 offen offset:1024 nt\n" \
  "s_add_u32 s44, s56, 71680\n" \
  "buffer_load_dwordx4 v[248:251], %3, %10, s44 offen nt\n" \
  "buffer_load_dwordx4 v[252:255], %3, %10, s44 offen offset:1024 nt\n" \
  "s_waitcnt vmcnt(42) lgkmcnt(0)\n" \
  "s_barrier\n" \
  "L_cfg_%=:\n" \
  "v_cmp_eq_u32 vcc, -1, v118\n" \
  "s_cbranch_vccz L_cfgok_%=\n" \
  "buffer_load_dword v118, v119, %10, s41 offen sc1\n" \
  "s_waitcnt vmcnt(0)\n" \
  "s_sub_u32 s40, s40, 1\n" \
  "s_cbranch_scc0 L_cfg_%=\n" \
  "s_mov_b32 s40, 0\n" \
  "v_mov_b32 v118, 0x1000\n" \
  "L_cfgok_%=:\n" \
  "s_nop 1\n" \
  "s_mov_b32 s64, 0xaaaaaaaa\n" \
  "s_mov_b32 s65, 0xaaaaaaaa\n" \
  "v_readlane_b32 s57, v118, 3\n" \
  "v_readlane_b32 s68, v118, 4\n" \
  "s_add_u32 s42, %13, 0\n" \
  "s_nop 0\n" \
  "v_readlane_b32 s47, v118, s42\n" \
  "v_readlane_b32 s48, v118, 2\n" \
  "s_nop 3\n" \
  "s_sub_u32 s68, s68, 4096\n" \
  "v_mov_b32 v80, s47\n" \
  "v_mov_b32 v81, s48\n" \
  "v_cndmask_b32 v76, v80, v81, s[64:65]\n" \
  "v_readlane_b32 s58, v118, 8\n" \
  "v_readlane_b32 s69, v118, 9\n" \
  "s_add_u32 s42, %13, 5\n" \
  "s_nop 0\n" \
  "v_readlane_b32 s47, v118, s42\n" \
  "v_readlane_b32 s48, v118, 7\n" \
  "s_nop 3\n" \
  "s_sub_u32 s69, s69, 4096\n" \
  "v_mov_b32 v80, s47\n" \
  "v_mov_b32 v81, s48\n" \
  "v_cndmask_b32 v77, v80, v81, s[64:65]\n" \
  "v_readlane_b32 s59, v118, 13\n" \
  "v_readlane_b32 s70, v118, 14\n" \
  "s_add_u32 s42, %13, 10\n" \
  "s_nop 0\n" \
  "v_readlane_b32 s47, v118, s42\n" \
  "v_readlane_b32 s48, v118, 12\n" \
  "s_nop 3\n" \
  "s_sub_u32 s70, s70, 4096\n" \
  "v_mov_b32 v80, s47\n" \
  "v_mov_b32 v81, s48\n" \
  "v_cndmask_b32 v78, v80, v81, s[64:65]\n" \
  "v_add_u32 v116, %0, v77\n" \
  "v_add_u32 v117, %0, v78\n" \
  "v_add_u32 %0, %0, v76\n" \
  "s_mov_b32 s38, 8000\n" \
  "s_memtime s[88:89]\n" \
  "s_memrealtime s[8:9]\n" \
  "s_nop 0\n" \
  "s_memrealtime s[10:11]\n" \
  "s_nop 0\n" \
  "s_memrealtime s[12:13]\n" \
  "s_nop 0\n" \
  "s_memrealtime s[14:15]\n" \
  "s_nop 0\n" \
  "s_memrealtime s[16:17]\n" \
  "s_nop 0\n" \
  "s_memrealtime s[18:19]\n" \
  "s_nop 0\n" \
  "s_memrealtime s[20:21]\n" \
  "s_nop 0\n" \
  "s_memrealtime s[22:23]\n" \
  "s_nop 0\n" \
  "s_memrealtime s[24:25]\n" \
  "s_nop 0\n" \
  "s_memrealtime s[26:27]\n" \
  "s_nop 0\n" \
  "s_memrealtime s[28:29]\n" \
  "s_nop 0\n" \
  "s_memrealtime s[30:31]\n" \
  "s_waitcnt lgkmcnt(0)\n" \
  "s_add_u32 s8, s8, s10\n" \
  "s_add_u32 s8, s8, s12\n" \
  "s_add_u32 s8, s8, s14\n" \
  "s_add_u32 s8, s8, s16\n" \
  "s_add_u32 s8, s8, s18\n" \
  "s_add_u32 s8, s8, s20\n" \
  "s_add_u32 s8, s8, s22\n" \
  "s_add_u32 s8, s8, s24\n" \
  "s_add_u32 s8, s8, s26\n" \
  "s_add_u32 s8, s8, s28\n" \
  "s_add_u32 s8, s8, s30\n" \
  "s_mov_b32 s36, s8\n" \
  "s_mov_b32 s37, s88\n" \
  "s_mov_b32 s66, 0x55ff55ff\n" \
  "s_mov_b32 s67, 0x55555555\n" \
  "s_mov_b32 s43, 0\n" \
  "s_mov_b32 s60, 0\n" \
  "v_mov_b32 v119, -1\n" \
  "s_mov_b64 exec, s[66:67]\n" \
  "buffer_store_dword v119, v116, %10, 0 offen sc1\n" \
  "s_mov_b64 exec, -1\n" \
  "s_mov_b32 s71, 1\n" \
  "s_mov_b32 s47, %c18+128\n" \
  "s_mov_b32 s48, 0\n" \
  "s_mov_b32 s49, 0x8000\n" \
  "s_mov_b32 s63, 0x10000\n" \
  "s_cmp_lt_u32 s90, 4096\n" \
  "s_cselect_b32 s44, %15, %15\n" \
  "s_add_u32 s50, s44, s90\n" \
  "s_cmp_lt_u32 s91, 4096\n" \
  "s_cselect_b32 s44, %15, %15\n" \
  "s_add_u32 s51, s44, s91\n" \
  "s_cmp_lt_u32 s92, 4096\n" \
  "s_cselect_b32 s44, %15, %15\n" \
  "s_add_u32 s52, s44, s92\n" \
  "s_cmp_lt_u32 s93, 4096\n" \
  "s_cselect_b32 s44, %15, %15\n" \
  "s_add_u32 s53, s44, s93\n" \
  "s_cmp_lt_u32 s94, 4096\n" \
  "s_cselect_b32 s44, %15, %15\n" \
  "s_add_u32 s54, s44, s94\n" \
  "s_cmp_lt_u32 s95, 4096\n" \
  "s_cselect_b32 s44, %15, %15\n" \
  "s_add_u32 s55, s44, s95\n" \
  "s_cmp_lt_u32 s96, 4096\n" \
  "s_cselect_b32 s44, %15, %15\n" \
  "s_add_u32 s56, s44, s96\n" \
  "v_mov_b32 v118, 1\n" \
  "s_waitcnt vmcnt(12)\n" \
  "s_mov_b64 exec, 1\n" \
  "buffer_store_dword v118, off, %10, %19 sc1\n" \
  "s_mov_b64 exec, -1\n" \
  "s_branch L_first_%=\n" \
  "L_again_%=:\n" \
  "s_cmp_lt_u32 s90, 4096\n" \
  "s_cselect_b32 s44, s57, s68\n" \
  "s_add_u32 s50, s44, s90\n" \
  "s_cmp_lt_u32 s91, 4096\n" \
  "s_cselect_b32 s44, s57, s68\n" \
  "s_add_u32 s51, s44, s91\n" \
  "s_cmp_lt_u32 s92, 4096\n" \
  "s_cselect_b32 s44, s57, s68\n" \
  "s_add_u32 s52, s44, s92\n" \
  "s_cmp_lt_u32 s93, 4096\n" \
  "s_cselect_b32 s44, s57, s68\n" \
  "s_add_u32 s53, s44, s93\n" \
  "s_cmp_lt_u32 s94, 4096\n" \
  "s_cselect_b32 s44, s57, s68\n" \
  "s_add_u32 s54, s44, s94\n" \
  "s_cmp_lt_u32 s95, 4096\n" \
  "s_cselect_b32 s44, s57, s68\n" \
  "s_add_u32 s55, s44, s95\n" \
  "s_cmp_lt_u32 s96, 4096\n" \
  "s_cselect_b32 s44, s57, s68\n" \
  "s_add_u32 s56, s44, s96\n" \
  "L_step_%=:\n" \
  "L_w0a_%=:\n" \
  "s_sub_u32 s39, s61, s36\n" \
  "s_add_u32 s61, s61, s41\n" \
  "s_add_u32 s71, s71, 1\n" \
  "s_cmp_gt_i32 s39, 0x20000\n" \
  "s_cbranch_scc1 L_w2a_%=\n" \
  "s_mul_i32 s39, s39, s38\n" \
  "s_ashr_i32 s39, s39, 12\n" \
  "s_add_u32 s39, s39, s37\n" \
  "L_w1a_%=:\n" \
  "s_memtime s[88:89]\n" \
  "s_waitcnt lgkmcnt(0)\n" \
  "s_sub_u32 s42, s39, s88\n" \
  "s_cmp_gt_i32 s42, 128\n" \
  "s_cbranch_scc1 L_w1a_%=\n" \
  "s_cmp_lt_i32 s42, -400\n" \
  "s_cbranch_scc1 L_w0a_%=\n" \
  "s_max_i32 s42, s42, 0\n" \
  "s_getpc_b64 s[88:89]\n" \
  "L_pca_%=:\n" \
  "s_andn2_b32 s42, s42, 3\n" \
  "s_sub_u32 s42, L_w2a_%=-L_pca_%=, s42\n" \
  "s_add_u32 s88, s88, s42\n" \
  "s_addc_u32 s89, s89, 0\n" \
  "s_setpc_b64 s[88:89]\n" \
  ".p2align 5\n" \
  "s_nop 0\n" \
  "s_nop 0\n" \
  "s_nop 0\n" \
  "s_nop 0\n" \
  "s_nop 0\n" \
  "s_nop 0\n" \
  "s_nop 0\n" \
  "s_nop 0\n" \
  "s_nop 0\n" \
  "s_nop 0\n" \
  "s_nop 0\n" \
  "s_nop 0\n" \
  "s_nop 0\n" \
  "s_nop 0\n" \
  "s_nop 0\n" \
  "s_nop 0\n" \
  "s_nop 0\n" \
  "s_nop 0\n" \
  "s_nop 0\n" \
  "s_nop 0\n" \
  "s_nop 0\n" \
  "s_nop 0\n" \
  "s_nop 0\n" \
  "s_nop 0\n" \
  "s_nop 0\n" \
  "s_nop 0\n" \
  "s_nop 0\n" \
  "s_nop 0\n" \
  "s_nop 0\n" \
  "s_nop 0\n" \
  "s_nop 0\n" \
  "s_nop 0\n" \
  "s_nop 0\n" \
  "s_nop 0\n" \
  "s_nop 0\n" \
  "s_nop 0\n" \
  "L_w2a_%=:\n" \
  "L_first_%=:\n" \
  "buffer_load_dwordx4 v[16:19], %3, %10, s50 offen sc1\n" \
  "s_cmp_lt_u32 s90, 4096\n" \
  "s_cselect_b32 s44, s58, s69\n" \
  "s_add_u32 s50, s44, s90\n" \
  "buffer_load_dwordx4 v[20:23], %3, %10, s51 offen sc1\n" \
  "s_cmp_lt_u32 s91, 4096\n" \
  "s_cselect_b32 s44, s58, s69\n" \
  "s_add_u32 s51, s44, s91\n" \
  "v_mov_b64 v[76:77], 0\n" \
  "v_mov_b64 v[78:79], 0\n" \
  "v_mov_b64 v[80:81], 0\n" \
  "v_mov_b64 v[82:83], 0\n" \
  "buffer_load_dwordx4 v[24:27], %3, %10, s52 offen sc1\n" \
  "s_cmp_lt_u32 s92, 4096\n" \
  "s_cselect_b32 s44, s58, s69\n" \
  "s_add_u32 s52, s44, s92\n" \
  "v_mov_b64 v[84:85], 0\n" \
  "v_mov_b64 v[86:87], 0\n" \
  "v_mov_b64 v[88:89], 0\n" \
  "v_mov_b64 v[90:91], 0\n" \
  "buffer_load_dwordx4 v[28:31], %3, %10, s53 offen sc1\n" \
  "s_cmp_lt_u32 s93, 4096\n" \
  "s_cselect_b32 s44, s58, s69\n" \
  "s_add_u32 s53, s44, s93\n" \
  "v_mov_b64 v[92:93], 0\n" \
  "v_mov_b64 v[94:95], 0\n" \
  "v_mov_b64 v[96:97], 0\n" \
  "v_mov_b64 v[98:99], 0\n" \
  "buffer_load_dwordx4 v[32:35], %3, %10, s54 offen sc1\n" \
  "s_cmp_lt_u32 s94, 4096\n" \
  "s_cselect_b32 s44, s58, s69\n" \
  "s_add_u32 s54, s44, s94\n" \
  "v_mov_b64 v[100:101], 0\n" \
  "v_mov_b64 v[102:103], 0\n" \
  "v_mov_b64 v[104:105], 0\n" \
  "v_mov_b64 v[106:107], 0\n" \
  "buffer_load_dwordx4 v[36:39], %3, %10, s55 offen sc1\n" \
  "s_cmp_lt_u32 s95, 4096\n" \
  "s_cselect_b32 s44, s58, s69\n" \
  "s_add_u32 s55, s44, s95\n" \
  "v_mov_b64 v[108:109], 0\n" \
  "v_mov_b64 v[110:111], 0\n" \
  "v_mov_b64 v[112:113], 0\n" \
  "v_mov_b64 v[114:115], 0\n" \
  "buffer_load_dwordx4 v[40:43], %3, %10, s56 offen sc1\n" \
  "s_cmp_lt_u32 s96, 4096\n" \
  "s_cselect_b32 s44, s58, s69\n" \
  "s_add_u32 s56, s44, s96\n" \
  "s_waitcnt vmcnt(6)\n" \
  "v_smfmac_f32_16x16x64_bf16 v[76:79], v[16:19], a[0:7], %9\n" \
  "v_smfmac_f32_16x16x64_bf16 v[80:83], v[16:19], a[56:63], %9\n" \
  "ds_write_b128 %4, v[16:19] offset:0\n" \
  "v_smfmac_f32_16x16x64_bf16 v[84:87], v[16:19], a[112:119], %9\n" \
  "v_smfmac_f32_16x16x64_bf16 v[88:91], v[16:19], a[168:175], %9\n" \
  "v_max_u32 v11, v16, v17\n" \
  "v_smfmac_f32_16x16x64_bf16 v[92:95], v[16:19], a[224:231], %9\n" \
  "v_max3_u32 v11, v11, v18, v19\n" \
  "v_smfmac_f32_16x16x64_bf16 v[96:99], v[16:19], v[144:151], %9\n" \
  "v_smfmac_f32_16x16x64_bf16 v[100:103], v[16:19], v[200:207], %9\n" \
  "s_waitcnt vmcnt(5)\n" \
  "v_smfmac_f32_16x16x64_bf16 v[76:79], v[20:23], a[8:15], %9\n" \
  "v_smfmac_f32_16x16x64_bf16 v[80:83], v[20:23], a[64:71], %9\n" \
  "ds_write_b128 %4, v[20:23] offset:1024\n" \
  "v_smfmac_f32_16x16x64_bf16 v[84:87], v[20:23], a[120:127], %9\n" \
  "v_smfmac_f32_16x16x64_bf16 v[88:91], v[20:23], a[176:183], %9\n" \
  "v_max3_u32 v11, v11, v20, v21\n" \
  "v_smfmac_f32_16x16x64_bf16 v[92:95], v[20:23], a[232:239], %9\n" \
  "v_max3_u32 v11, v11, v22, v23\n" \
  "v_smfmac_f32_16x16x64_bf16 v[96:99], v[20:23], v[152:159], %9\n" \
  "v_smfmac_f32_16x16x64_bf16 v[100:103], v[20:23], v[208:215], %9\n" \
  "s_waitcnt vmcnt(4)\n" \
  "v_smfmac_f32_16x16x64_bf16 v[76:79], v[24:27], a[16:23], %9\n" \
  "v_smfmac_f32_16x16x64_bf16 v[80:83], v[24:27], a[72:79], %9\n" \
  "ds_write_b128 %4, v[24:27] offset:2048\n" \
  "v_smfmac_f32_16x16x64_bf16 v[84:87], v[24:27], a[128:135], %9\n" \
  "v_smfmac_f32_16x16x64_bf16 v[88:91], v[24:27], a[184:191], %9\n" \
  "v_max3_u32 v11, v11, v24, v25\n" \
  "v_smfmac_f32_16x16x64_bf16 v[92:95], v[24:27], a[240:247], %9\n" \
  "v_max3_u32 v11, v11, v26, v27\n" \
  "v_smfmac_f32_16x16x64_bf16 v[96:99], v[24:27], v[160:167], %9\n" \
  "v_smfmac_f32_16x16x64_bf16 v[100:103], v[24:27], v[216:223], %9\n" \
  "s_waitcnt vmcnt(3)\n" \
  "v_smfmac_f32_16x16x64_bf16 v[76:79], v[28:31], a[24:31], %9\n" \
  "ds_read_b128 v[12:15], %5 offset:0\n" \
  "v_smfmac_f32_16x16x64_bf16 v[80:83], v[28:31], a[80:87], %9\n" \
  "ds_read_b128 v[16:19], %5 offset:512\n" \
  "s_waitcnt vmcnt(2)\n" \
  "v_smfmac_f32_16x16x64_bf16 v[76:79], v[32:35], a[32:39], %9\n" \
  "ds_read_b128 v[20:23], %5 offset:1024\n" \
  "s_waitcnt vmcnt(1)\n" \
  "v_smfmac_f32_16x16x64_bf16 v[76:79], v[36:39], a[40:47], %9\n" \
  "ds_read_b128 v[24:27], %5 offset:1536\n" \
  "v_smfmac_f32_16x16x64_bf16 v[80:83], v[32:35], a[88:95], %9\n" \
  "s_waitcnt vmcnt(0)\n" \
  "v_smfmac_f32_16x16x64_bf16 v[76:79], v[40:43], a[48:55], %9\n" \
  "s_nop 1\n" \
  "v_smfmac_f32_16x16x64_bf16 v[80:83], v[36:39], a[96:103], %9\n" \
  "s_nop 1\n" \
  "v_smfmac_f32_16x16x64_bf16 v[80:83], v[40:43], a[104:111], %9\n" \
  "s_nop 1\n" \
  "v_smfmac_f32_16x16x64_bf16 v[84:87], v[28:31], a[136:143], %9\n" \
  "v_add_f32 v76, v76, v78\n" \
  "v_smfmac_f32_16x16x64_bf16 v[84:87], v[32:35], a[144:151], %9\n" \
  "v_add_f32 v77, v77, v79\n" \
  "v_smfmac_f32_16x16x64_bf16 v[84:87], v[36:39], a[152:159], %9\n" \
  "v_cmp_u_f32 vcc, v76, v77\n" \
  "v_smfmac_f32_16x16x64_bf16 v[84:87], v[40:43], a[160:167], %9\n" \
  "v_cvt_pk_bf16_f32 v78, v76, v77\n" \
  "v_smfmac_f32_16x16x64_bf16 v[88:91], v[28:31], a[192:199], %9\n" \
  "ds_write_b32 %6, v78 offset:0\n" \
  "v_smfmac_f32_16x16x64_bf16 v[88:91], v[32:35], a[200:207], %9\n" \
  "s_cbranch_vccnz L_nan_%=\n" \
  "L_go_%=:\n" \
  "v_add_f32 v80, v80, v82\n" \
  "v_smfmac_f32_16x16x64_bf16 v[88:91], v[36:39], a[208:215], %9\n" \
  "v_add_f32 v81, v81, v83\n" \
  "v_smfmac_f32_16x16x64_bf16 v[88:91], v[40:43], a[216:223], %9\n" \
  "v_cvt_pk_bf16_f32 v82, v80, v81\n" \
  "v_smfmac_f32_16x16x64_bf16 v[92:95], v[28:31], a[248:255], %9\n" \
  "ds_write_b32 %6, v82 offset:4\n" \
  "v_smfmac_f32_16x16x64_bf16 v[92:95], v[32:35], v[120:127], %9\n" \
  "v_add_f32 v84, v84, v86\n" \
  "v_smfmac_f32_16x16x64_bf16 v[92:95], v[36:39], v[128:135], %9\n" \
  "v_add_f32 v85, v85, v87\n" \
  "v_smfmac_f32_16x16x64_bf16 v[92:95], v[40:43], v[136:143], %9\n" \
  "v_cvt_pk_bf16_f32 v86, v84, v85\n" \
  "v_smfmac_f32_16x16x64_bf16 v[96:99], v[28:31], v[168:175], %9\n" \
  "ds_write_b32 %6, v86 offset:640\n" \
  "v_smfmac_f32_16x16x64_bf16 v[96:99], v[32:35], v[176:183], %9\n" \
  "v_add_f32 v88, v88, v90\n" \
  "v_smfmac_f32_16x16x64_bf16 v[96:99], v[36:39], v[184:191], %9\n" \
  "v_add_f32 v89, v89, v91\n" \
  "v_smfmac_f32_16x16x64_bf16 v[96:99], v[40:43], v[192:199], %9\n" \
  "v_cvt_pk_bf16_f32 v90, v88, v89\n" \
  "v_smfmac_f32_16x16x64_bf16 v[100:103], v[28:31], v[224:231], %9\n" \
  "ds_write_b32 %6, v90 offset:644\n" \
  "v_smfmac_f32_16x16x64_bf16 v[100:103], v[32:35], v[232:239], %9\n" \
  "ds_read_b64_tr_b16 v[76:77], %8\n" \
  "v_smfmac_f32_16x16x64_bf16 v[100:103], v[36:39], v[240:247], %9\n" \
  "ds_read_b64_tr_b16 v[78:79], %8 offset:160\n" \
  "v_smfmac_f32_16x16x64_bf16 v[100:103], v[40:43], v[248:255], %9\n" \
  "v_add_f32 v92, v92, v94\n" \
  "v_add_f32 v93, v93, v95\n" \
  "v_cvt_pk_bf16_f32 v94, v92, v93\n" \
  "ds_write_b32 %6, v94 offset:1280\n" \
  "v_pk_add_f32 v[96:97], v[96:97], v[98:99]\n" \
  "s_nop 0\n" \
  "v_cvt_pk_bf16_f32 v98, v96, v97\n" \
  "ds_write_b32 %6, v98 offset:1284\n" \
  "v_pk_add_f32 v[100:101], v[100:101], v[102:103]\n" \
  "s_nop 0\n" \
  "v_cvt_pk_bf16_f32 v100, v100, v101\n" \
  "ds_write_b32 %7, v100\n" \
  "ds_read_b64_tr_b16 v[80:81], %8 offset:1280\n" \
  "ds_read_b64_tr_b16 v[82:83], %8 offset:1440\n" \
  "s_waitcnt lgkmcnt(5)\n" \
  "v_smfmac_f32_16x16x64_bf16 v[104:107], v[76:79], v[44:51], %9\n" \
  "v_smfmac_f32_16x16x64_bf16 v[108:111], v[76:79], v[60:67], %9\n" \
  "v_smfmac_f32_16x16x64_bf16 v[112:115], v[76:79], v[12:19], %9\n" \
  "s_waitcnt lgkmcnt(0)\n" \
  "v_smfmac_f32_16x16x64_bf16 v[104:107], v[80:83], v[52:59], %9\n" \
  "v_smfmac_f32_16x16x64_bf16 v[108:111], v[80:83], v[68:75], %9\n" \
  "v_smfmac_f32_16x16x64_bf16 v[112:115], v[80:83], v[20:27], %9\n" \
  "v_add_u32 v15, s49, %4\n" \
  "s_nop 4\n" \
  "v_pk_add_f32 v[104:105], v[104:105], v[106:107]\n" \
  "v_pk_add_f32 v[108:109], v[108:109], v[110:111]\n" \
  "v_pk_add_f32 v[112:113], v[112:113], v[114:115]\n" \
  "ds_write2st64_b32 %1, v104, v105 offset0:0 offset1:1\n" \
  "ds_write2st64_b32 %1, v108, v109 offset0:2 offset1:3\n" \
  "ds_write2st64_b32 %1, v112, v113 offset0:4 offset1:5\n" \
  "s_barrier\n" \
  "L_sread_%=:\n" \
  "ds_read_b128 v[28:31], v15\n" \
  "ds_read_b128 v[32:35], %2\n" \
  "v_mov_b64 v[40:41], -1\n" \
  "v_mov_b64 v[42:43], -1\n" \
  "s_waitcnt lgkmcnt(1)\n" \
  "v_pk_add_f32 v[36:37], v[28:29], v[30:31]\n" \
  "s_waitcnt lgkmcnt(0)\n" \
  "v_pk_add_f32 v[38:39], v[32:33], v[34:35]\n" \
  "v_add_f32 v84, v36, v37\n" \
  "v_add_f32 v85, v38, v39\n" \
  "L_sexp_%=:\n" \
  "v_exp_f32 v88, v84\n" \
  "v_exp_f32 v89, v85\n" \
  "v_add_f32 v86, |v84|, |v85|\n" \
  "v_fmamk_f32 v90, v89, 0x3e800000, v88\n" \
  "v_mov_b32_dpp v91, v88 quad_perm:[1,0,3,2] row_mask:0xf bank_mask:0xf\n" \
  "v_mov_b32_dpp v92, v89 quad_perm:[1,0,3,2] row_mask:0xf bank_mask:0xf\n" \
  "v_add_f32_dpp v90, v90, v90 row_ror:8 row_mask:0xf bank_mask:0xf\n" \
  "v_cmp_nge_f32 vcc, 0x42200000, v86\n" \
  "v_cndmask_b32 v88, v88, v92, s[64:65]\n" \
  "v_add_f32_dpp v90, v90, v90 row_ror:4 row_mask:0xf bank_mask:0xf\n" \
  "v_cndmask_b32 v89, v91, v89, s[64:65]\n" \
  "s_nop 0\n" \
  "v_add_f32_dpp v90, v90, v90 row_ror:2 row_mask:0xf bank_mask:0xf\n" \
  "s_nop 1\n" \
  "v_add_f32_dpp v87, v90, v90 row_ror:1 row_mask:0xf bank_mask:0xf\n" \
  "v_add_f32_dpp v90, v90, v90 row_ror:1 row_mask:0xf bank_mask:0xf\n" \
  "s_cbranch_vccnz L_smax_%=\n" \
  "L_sok_%=:\n" \
  "s_nop 0\n" \
  "v_permlane32_swap_b32 v90, v87\n" \
  "v_add_f32 v90, v90, v87\n" \
  "v_rcp_f32 v90, v90\n" \
  "s_mov_b64 exec, s[66:67]\n" \
  "v_mul_f32 v88, v88, v90\n" \
  "v_mul_f32 v89, v89, v90\n" \
  "v_cvt_pk_bf16_f32 v88, v88, |v89|\n" \
  "buffer_store_dword v88, %0, %10, 0 offen sc1\n" \
  "s_nop 7\n" \
  "buffer_store_dword v40, v117, %10, 0 offen sc1\n" \
  "s_mov_b64 exec, -1\n" \
  "s_mov_b32 s60, 0\n" \
  "s_add_u32 s43, s43, 1\n" \
  "s_cmp_lt_u32 s43, 63\n" \
  "s_cbranch_scc0 L_end_%=\n" \
  "ds_write_b128 v15, v[40:43]\n" \
  "ds_write_b128 %2, v[40:43]\n" \
  "v_xor_b32 %1, 0x4000, %1\n" \
  "v_xor_b32 %2, 0x4000, %2\n" \
  "s_xor_b32 s49, s49, 0x4000\n" \
  "s_cmp_lt_u32 %12, 2\n" \
  "s_cbranch_scc0 L_l1_%=\n" \
  "s_memtime s[88:89]\n" \
  "s_memrealtime s[8:9]\n" \
  "s_nop 0\n" \
  "s_memrealtime s[10:11]\n" \
  "s_nop 0\n" \
  "s_memrealtime s[12:13]\n" \
  "s_nop 0\n" \
  "s_memrealtime s[14:15]\n" \
  "s_nop 0\n" \
  "s_memrealtime s[16:17]\n" \
  "s_nop 0\n" \
  "s_memrealtime s[18:19]\n" \
  "s_nop 0\n" \
  "s_memrealtime s[20:21]\n" \
  "s_nop 0\n" \
  "s_memrealtime s[22:23]\n" \
  "s_nop 0\n" \
  "s_memrealtime s[24:25]\n" \
  "s_nop 0\n" \
  "s_memrealtime s[26:27]\n" \
  "s_nop 0\n" \
  "s_memrealtime s[28:29]\n" \
  "s_nop 0\n" \
  "s_memrealtime s[30:31]\n" \
  "s_waitcnt lgkmcnt(0)\n" \
  "s_add_u32 s8, s8, s10\n" \
  "s_add_u32 s8, s8, s12\n" \
  "s_add_u32 s8, s8, s14\n" \
  "s_add_u32 s8, s8, s16\n" \
  "s_add_u32 s8, s8, s18\n" \
  "s_add_u32 s8, s8, s20\n" \
  "s_add_u32 s8, s8, s22\n" \
  "s_add_u32 s8, s8, s24\n" \
  "s_add_u32 s8, s8, s26\n" \
  "s_add_u32 s8, s8, s28\n" \
  "s_add_u32 s8, s8, s30\n" \
  "s_sub_u32 s42, s8, s36\n" \
  "s_cmp_lt_u32 s42, 2600\n" \
  "s_cbranch_scc0 L_lts_%=\n" \
  "s_mul_i32 s42, s42, s38\n" \
  "s_lshr_b32 s42, s42, 12\n" \
  "s_sub_u32 s44, s88, s37\n" \
  "s_sub_u32 s42, s44, s42\n" \
  "s_max_i32 s42, s42, -100\n" \
  "s_min_i32 s42, s42, 100\n" \
  "s_add_u32 s38, s38, s42\n" \
  "L_lts_%=:\n" \
  "s_mov_b32 s36, s8\n" \
  "s_mov_b32 s37, s88\n" \
  "L_l1_%=:\n" \
  "buffer_load_dwordx4 v[44:47], %3, %10, s46 offen offset:0\n" \
  "buffer_load_dwordx4 v[48:51], %3, %10, s46 offen offset:1024\n" \
  "buffer_load_dwordx4 v[52:55], %3, %10, s46 offen offset:2048\n" \
  "v_mov_b32 v118, %0\n" \
  "v_mov_b32 %0, v116\n" \
  "v_mov_b32 v116, v117\n" \
  "v_mov_b32 v117, v118\n" \
  "s_mov_b32 s44, s57\n" \
  "s_mov_b32 s57, s58\n" \
  "s_mov_b32 s58, s59\n" \
  "s_mov_b32 s59, s44\n" \
  "s_mov_b32 s44, s68\n" \
  "s_mov_b32 s68, s69\n" \
  "s_mov_b32 s69, s70\n" \
  "s_mov_b32 s70, s44\n" \
  "buffer_load_dwordx4 v[56:59], %3, %10, s46 offen offset:3072\n" \
  "v_readfirstlane_b32 s44, v119\n" \
  "s_nop 0\n" \
  "s_and_b32 s42, s44, 1023\n" \
  "s_sub_u32 s42, s71, s42\n" \
  "s_cbranch_scc1 L_noap_%=\n" \
  "s_cmp_eq_u32 s44, -1\n" \
  "s_cbranch_scc1 L_noap_%=\n" \
  "s_bfe_u32 s88, s44, 0xb000a\n" \
  "s_sub_u32 s88, s88, 512\n" \
  "s_mul_i32 s42, s42, s88\n" \
  "s_add_u32 s61, s61, s42\n" \
  "s_add_u32 s41, s41, s88\n" \
  "s_add_u32 s47, s47, 4\n" \
  "L_noap_%=:\n" \
  "s_add_u32 s46, s46, 4096\n" \
  "buffer_load_dwordx4 v[60:63], %3, %10, s46 offen offset:0\n" \
  "s_sub_u32 s42, s71, s43\n" \
  "s_cmp_eq_u32 s42, s48\n" \
  "s_cbranch_scc1 L_nolead_%=\n" \
  "s_mov_b32 s48, s42\n" \
  "s_cmp_lg_u32 %11, 0\n" \
  "s_cbranch_scc1 L_nolead_%=\n" \
  "s_or_b32 s42, s42, s63\n" \
  "s_mov_b32 s63, 0x10000\n" \
  "v_mov_b32 v118, s42\n" \
  "s_mov_b32 s42, %c18+256\n" \
  "s_mov_b64 exec, 1\n" \
  "buffer_store_dword v118, off, %10, s42 sc1\n" \
  "s_mov_b64 exec, -1\n" \
  "L_nolead_%=:\n" \
  "buffer_load_dwordx4 v[64:67], %3, %10, s46 offen offset:1024\n" \
  "buffer_load_dwordx4 v[68:71], %3, %10, s46 offen offset:2048\n" \
  "buffer_load_dwordx4 v[72:75], %3, %10, s46 offen offset:3072\n" \
  "s_add_u32 s46, s46, 28672\n" \
  "buffer_load_dword v119, off, %10, s47 sc1\n" \
  "s_cmp_lt_u32 %12, 2\n" \
  "s_cbranch_scc1 L_l2_%=\n" \
  "s_memtime s[88:89]\n" \
  "s_memrealtime s[8:9]\n" \
  "s_nop 0\n" \
  "s_memrealtime s[10:11]\n" \
  "s_nop 0\n" \
  "s_memrealtime s[12:13]\n" \
  "s_nop 0\n" \
  "s_memrealtime s[14:15]\n" \
  "s_nop 0\n" \
  "s_memrealtime s[16:17]\n" \
  "s_nop 0\n" \
  "s_memrealtime s[18:19]\n" \
  "s_nop 0\n" \
  "s_memrealtime s[20:21]\n" \
  "s_nop 0\n" \
  "s_memrealtime s[22:23]\n" \
  "s_nop 0\n" \
  "s_memrealtime s[24:25]\n" \
  "s_nop 0\n" \
  "s_memrealtime s[26:27]\n" \
  "s_nop 0\n" \
  "s_memrealtime s[28:29]\n" \
  "s_nop 0\n" \
  "s_memrealtime s[30:31]\n" \
  "s_waitcnt lgkmcnt(0)\n" \
  "s_add_u32 s8, s8, s10\n" \
  "s_add_u32 s8, s8, s12\n" \
  "s_add_u32 s8, s8, s14\n" \
  "s_add_u32 s8, s8, s16\n" \
  "s_add_u32 s8, s8, s18\n" \
  "s_add_u32 s8, s8, s20\n" \
  "s_add_u32 s8, s8, s22\n" \
  "s_add_u32 s8, s8, s24\n" \
  "s_add_u32 s8, s8, s26\n" \
  "s_add_u32 s8, s8, s28\n" \
  "s_add_u32 s8, s8, s30\n" \
  "s_sub_u32 s42, s8, s36\n" \
  "s_cmp_lt_u32 s42, 2600\n" \
  "s_cbranch_scc0 L_ltt_%=\n" \
  "s_mul_i32 s42, s42, s38\n" \
  "s_lshr_b32 s42, s42, 12\n" \
  "s_sub_u32 s44, s88, s37\n" \
  "s_sub_u32 s42, s44, s42\n" \
  "s_max_i32 s42, s42, -100\n" \
  "s_min_i32 s42, s42, 100\n" \
  "s_add_u32 s38, s38, s42\n" \
  "L_ltt_%=:\n" \
  "s_mov_b32 s36, s8\n" \
  "s_mov_b32 s37, s88\n" \
  "L_l2_%=:\n" \
  "s_cmp_eq_u32 s43, 1\n" \
  "s_cbranch_scc0 L_step_%=\n" \
  "s_mov_b32 s44, %c18+384\n" \
  "L_an_%=:\n" \
  "buffer_load_dwordx4 v[76:79], off, %10, s44 sc1\n" \
  "s_waitcnt vmcnt(0)\n" \
  "v_readfirstlane_b32 s61, v76\n" \
  "v_readfirstlane_b32 s41, v77\n" \
  "v_readfirstlane_b32 s45, v78\n" \
  "v_readfirstlane_b32 s38, v79\n" \
  "s_nop 0\n" \
  "s_max_u32 s42, s61, s41\n" \
  "s_max_u32 s42, s42, s45\n" \
  "s_max_u32 s42, s42, s38\n" \
  "s_cmp_lg_u32 s42, -1\n" \
  "s_cbranch_scc1 L_anok_%=\n" \
  "s_sub_u32 s40, s40, 1\n" \
  "s_cbranch_scc0 L_an_%=\n" \
  "s_mov_b32 s40, 0\n" \
  "s_mov_b32 s41, 2048\n" \
  "s_mov_b32 s45, 1024\n" \
  "s_mov_b32 s38, 8000\n" \
  "L_anok_%=:\n" \
  "s_sub_u32 s42, s61, s36\n" \
  "s_add_u32 s42, s42, 0x20000\n" \
  "s_cmp_lt_u32 s42, 0x40000\n" \
  "s_cselect_b32 s61, s61, s36\n" \
  "s_add_u32 s61, s61, s41\n" \
  "s_sub_u32 s39, s61, s36\n" \
  "s_mul_i32 s39, s39, s38\n" \
  "s_ashr_i32 s39, s39, 12\n" \
  "s_add_u32 s39, s39, s37\n" \
  "L_cwf_%=:\n" \
  "s_memtime s[88:89]\n" \
  "s_waitcnt lgkmcnt(0)\n" \
  "s_sub_u32 s42, s39, s88\n" \
  "s_cmp_gt_i32 s42, 700\n" \
  "s_cbranch_scc1 L_cwf_%=\n" \
  "s_memtime s[88:89]\n" \
  "s_memrealtime s[8:9]\n" \
  "s_nop 0\n" \
  "s_memrealtime s[10:11]\n" \
  "s_nop 0\n" \
  "s_memrealtime s[12:13]\n" \
  "s_nop 0\n" \
  "s_memrealtime s[14:15]\n" \
  "s_nop 0\n" \
  "s_memrealtime s[16:17]\n" \
  "s_nop 0\n" \
  "s_memrealtime s[18:19]\n" \
  "s_nop 0\n" \
  "s_memrealtime s[20:21]\n" \
  "s_nop 0\n" \
  "s_memrealtime s[22:23]\n" \
  "s_nop 0\n" \
  "s_memrealtime s[24:25]\n" \
  "s_nop 0\n" \
  "s_memrealtime s[26:27]\n" \
  "s_nop 0\n" \
  "s_memrealtime s[28:29]\n" \
  "s_nop 0\n" \
  "s_memrealtime s[30:31]\n" \
  "s_waitcnt lgkmcnt(0)\n" \
  "s_add_u32 s8, s8, s10\n" \
  "s_add_u32 s8, s8, s12\n" \
  "s_add_u32 s8, s8, s14\n" \
  "s_add_u32 s8, s8, s16\n" \
  "s_add_u32 s8, s8, s18\n" \
  "s_add_u32 s8, s8, s20\n" \
  "s_add_u32 s8, s8, s22\n" \
  "s_add_u32 s8, s8, s24\n" \
  "s_add_u32 s8, s8, s26\n" \
  "s_add_u32 s8, s8, s28\n" \
  "s_add_u32 s8, s8, s30\n" \
  "s_mov_b32 s36, s8\n" \
  "s_mov_b32 s37, s88\n" \
  "s_branch L_step_%=\n" \
  "L_smax_%=:\n" \
  "s_cmp_eq_u32 s60, 1\n" \
  "s_cbranch_scc1 L_sok_%=\n" \
  "v_max_u32 v86, v28, v29\n" \
  "v_max3_u32 v86, v86, v30, v31\n" \
  "v_max3_u32 v86, v86, v32, v33\n" \
  "v_max3_u32 v86, v86, v34, v35\n" \
  "v_cmp_eq_u32 vcc, -1, v86\n" \
  "s_cbranch_vccz L_smax2_%=\n" \
  "s_add_u32 s60, s60, 2\n" \
  "s_cmp_lt_u32 s60, 64\n" \
  "s_cbranch_scc1 L_sread_%=\n" \
  "L_smax2_%=:\n" \
  "s_mov_b32 s60, 1\n" \
  "v_max_f32 v86, v84, v85\n" \
  "s_nop 1\n" \
  "v_max_f32_dpp v86, v86, v86 row_ror:8 row_mask:0xf bank_mask:0xf\n" \
  "s_nop 1\n" \
  "v_max_f32_dpp v86, v86, v86 row_ror:4 row_mask:0xf bank_mask:0xf\n" \
  "s_nop 1\n" \
  "v_max_f32_dpp v86, v86, v86 row_ror:2 row_mask:0xf bank_mask:0xf\n" \
  "s_nop 1\n" \
  "v_max_f32_dpp v86, v86, v86 row_ror:1 row_mask:0xf bank_mask:0xf\n" \
  "v_mov_b32 v87, v86\n" \
  "s_nop 1\n" \
  "v_permlane32_swap_b32 v86, v87\n" \
  "v_max_f32 v86, v86, v87\n" \
  "v_sub_f32 v84, v84, v86\n" \
  "v_sub_f32 v85, v85, v86\n" \
  "s_branch L_sexp_%=\n" \
  "L_purge_%=:\n" \
  "v_and_b32 v118, v28, v32\n" \
  "v_and_b32 v118, v118, v36\n" \
  "v_and_b32 v118, v118, v40\n" \
  "v_cmp_ne_u32 vcc, -1, v118\n" \
  "s_nop 1\n" \
  "s_cmp_lg_u64 vcc, 0\n" \
  "s_cselect_b32 s63, 0x10000, 0\n" \
  "s_sub_u32 s39, s61, s36\n" \
  "s_mul_i32 s39, s39, s38\n" \
  "s_ashr_i32 s39, s39, 12\n" \
  "s_add_u32 s39, s39, s37\n" \
  "L_cwp_%=:\n" \
  "s_memtime s[88:89]\n" \
  "s_waitcnt lgkmcnt(0)\n" \
  "s_sub_u32 s42, s39, s88\n" \
  "s_cmp_gt_i32 s42, 720\n" \
  "s_cbranch_scc1 L_cwp_%=\n" \
  "s_memtime s[88:89]\n" \
  "s_memrealtime s[8:9]\n" \
  "s_nop 0\n" \
  "s_memrealtime s[10:11]\n" \
  "s_nop 0\n" \
  "s_memrealtime s[12:13]\n" \
  "s_nop 0\n" \
  "s_memrealtime s[14:15]\n" \
  "s_nop 0\n" \
  "s_memrealtime s[16:17]\n" \
  "s_nop 0\n" \
  "s_memrealtime s[18:19]\n" \
  "s_nop 0\n" \
  "s_memrealtime s[20:21]\n" \
  "s_nop 0\n" \
  "s_memrealtime s[22:23]\n" \
  "s_nop 0\n" \
  "s_memrealtime s[24:25]\n" \
  "s_nop 0\n" \
  "s_memrealtime s[26:27]\n" \
  "s_nop 0\n" \
  "s_memrealtime s[28:29]\n" \
  "s_nop 0\n" \
  "s_memrealtime s[30:31]\n" \
  "s_waitcnt lgkmcnt(0)\n" \
  "s_add_u32 s8, s8, s10\n" \
  "s_add_u32 s8, s8, s12\n" \
  "s_add_u32 s8, s8, s14\n" \
  "s_add_u32 s8, s8, s16\n" \
  "s_add_u32 s8, s8, s18\n" \
  "s_add_u32 s8, s8, s20\n" \
  "s_add_u32 s8, s8, s22\n" \
  "s_add_u32 s8, s8, s24\n" \
  "s_add_u32 s8, s8, s26\n" \
  "s_add_u32 s8, s8, s28\n" \
  "s_add_u32 s8, s8, s30\n" \
  "s_mov_b32 s36, s8\n" \
  "s_mov_b32 s37, s88\n" \
  "s_sub_u32 s39, s61, s36\n" \
  "s_mul_i32 s39, s39, s38\n" \
  "s_ashr_i32 s39, s39, 12\n" \
  "s_add_u32 s39, s39, s37\n" \
  "L_cwq_%=:\n" \
  "s_memtime s[88:89]\n" \
  "s_waitcnt lgkmcnt(0)\n" \
  "s_sub_u32 s42, s39, s88\n" \
  "s_cmp_gt_i32 s42, 420\n" \
  "s_cbranch_scc1 L_cwq_%=\n" \
  "v_readlane_b32 s42, %0, 0\n" \
  "v_readlane_b32 s44, %0, 1\n" \
  "s_add_u32 s88, %12, 1\n" \
  "s_lshr_b32 s88, s88, 1\n" \
  "s_lshl_b32 s88, s88, 5\n" \
  "s_sub_u32 s44, s44, s88\n" \
  "s_lshl_b32 s88, s88, 2\n" \
  "s_sub_u32 s42, s42, s88\n" \
  "v_lshrrev_b32 v118, 2, %3\n" \
  "v_mov_b32 v11, -1\n" \
  "buffer_store_dword v11, v118, %10, s42 offen sc1\n" \
  "buffer_store_dword v11, v118, %10, s42 offen offset:256 sc1\n" \
  "s_mov_b32 exec_hi, 0\n" \
  "buffer_store_dword v11, v118, %10, s44 offen sc1\n" \
  "s_mov_b64 exec, -1\n" \
  "s_branch L_again_%=\n" \
  "L_nan_%=:\n" \
  "v_max3_u32 v11, v11, v28, v29\n" \
  "v_max3_u32 v11, v11, v30, v31\n" \
  "v_max3_u32 v11, v11, v32, v33\n" \
  "v_max3_u32 v11, v11, v34, v35\n" \
  "v_max3_u32 v11, v11, v36, v37\n" \
  "v_max3_u32 v11, v11, v38, v39\n" \
  "v_max3_u32 v11, v11, v40, v41\n" \
  "v_max3_u32 v11, v11, v42, v43\n" \
  "v_cmp_eq_u32 vcc, -1, v11\n" \
  "s_cbranch_vccz L_go_%=\n" \
  "s_sub_u32 s40, s40, 1\n" \
  "s_cbranch_scc0 L_purge_%=\n" \
  "s_mov_b32 s40, 0\n" \
  "s_branch L_go_%=\n" \
  "L_end_%=:\n" \
  "s_cmp_lg_u32 %11, 0\n" \
  "s_cbranch_scc1 L_e2_%=\n" \
  "v_mov_b32 v118, 0x7fffffff\n" \
  "s_mov_b32 s42, %c18+256\n" \
  "s_mov_b64 exec, 1\n" \
  "buffer_store_dword v118, off, %10, s42 sc1\n" \
  "L_e2_%=:\n" \
  "s_endpgm\n"
#define SCORE_CLOB \
  "v11", "v12", "v13", "v14", "v15", "v16", "v17", "v18", "v19", "v20", "v21", "v22", "v23", "v24", "v25", "v26", "v27", "v28", "v29", "v30", \
  "v31", "v32", "v33", "v34", "v35", "v36", "v37", "v38", "v39", "v40", "v41", "v42", "v43", "v44", "v45", "v46", "v47", "v48", "v49", "v50", \
  "v51", "v52", "v53", "v54", "v55", "v56", "v57", "v58", "v59", "v60", "v61", "v62", "v63", "v64", "v65", "v66", "v67", "v68", "v69", "v70", \
  "v71", "v72", "v73", "v74", "v75", "v76", "v77", "v78", "v79", "v80", "v81", "v82", "v83", "v84", "v85", "v86", "v87", "v88", "v89", "v90", \
  "v91", "v92", "v93", "v94", "v95", "v96", "v97", "v98", "v99", "v100", "v101", "v102", "v103", "v104", "v105", "v106", "v107", "v108", "v109", "v110", \
  "v111", "v112", "v113", "v114", "v115", "v116", "v117", "v118", "v119", "v120", "v121", "v122", "v123", "v124", "v125", "v126", "v127", "v128", "v129", "v130", \
  "v131", "v132", "v133", "v134", "v135", "v136", "v137", "v138", "v139", "v140", "v141", "v142", "v143", "v144", "v145", "v146", "v147", "v148", "v149", "v150", \
  "v151", "v152", "v153", "v154", "v155", "v156", "v157", "v158", "v159", "v160", "v161", "v162", "v163", "v164", "v165", "v166", "v167", "v168", "v169", "v170", \
  "v171", "v172", "v173", "v174", "v175", "v176", "v177", "v178", "v179", "v180", "v181", "v182", "v183", "v184", "v185", "v186", "v187", "v188", "v189", "v190", \
  "v191", "v192", "v193", "v194", "v195", "v196", "v197", "v198", "v199", "v200", "v201", "v202", "v203", "v204", "v205", "v206", "v207", "v208", "v209", "v210", \
  "v211", "v212", "v213", "v214", "v215", "v216", "v217", "v218", "v219", "v220", "v221", "v222", "v223", "v224", "v225", "v226", "v227", "v228", "v229", "v230", \
  "v231", "v232", "v233", "v234", "v235", "v236", "v237", "v238", "v239", "v240", "v241", "v242", "v243", "v244", "v245", "v246", "v247", "v248", "v249", "v250", \
  "v251", "v252", "v253", "v254", "v255", "a0", "a1", "a2", "a3", "a4", "a5", "a6", "a7", "a8", "a9", "a10", "a11", "a12", "a13", "a14", \
  "a15", "a16", "a17", "a18", "a19", "a20", "a21", "a22", "a23", "a24", "a25", "a26", "a27", "a28", "a29", "a30", "a31", "a32", "a33", "a34", \
  "a35", "a36", "a37", "a38", "a39", "a40", "a41", "a42", "a43", "a44", "a45", "a46", "a47", "a48", "a49", "a50", "a51", "a52", "a53", "a54", \
  "a55", "a56", "a57", "a58", "a59", "a60", "a61", "a62", "a63", "a64", "a65", "a66", "a67", "a68", "a69", "a70", "a71", "a72", "a73", "a74", \
  "a75", "a76", "a77", "a78", "a79", "a80", "a81", "a82", "a83", "a84", "a85", "a86", "a87", "a88", "a89", "a90", "a91", "a92", "a93", "a94", \
  "a95", "a96", "a97", "a98", "a99", "a100", "a101", "a102", "a103", "a104", "a105", "a106", "a107", "a108", "a109", "a110", "a111", "a112", "a113", "a114", \
  "a115", "a116", "a117", "a118", "a119", "a120", "a121", "a122", "a123", "a124", "a125", "a126", "a127", "a128", "a129", "a130", "a131", "a132", "a133", "a134", \
  "a135", "a136", "a137", "a138", "a139", "a140", "a141", "a142", "a143", "a144", "a145", "a146", "a147", "a148", "a149", "a150", "a151", "a152", "a153", "a154", \
  "a155", "a156", "a157", "a158", "a159", "a160", "a161", "a162", "a163", "a164", "a165", "a166", "a167", "a168", "a169", "a170", "a171", "a172", "a173", "a174", \
  "a175", "a176", "a177", "a178", "a179", "a180", "a181", "a182", "a183", "a184", "a185", "a186", "a187", "a188", "a189", "a190", "a191", "a192", "a193", "a194", \
  "a195", "a196", "a197", "a198", "a199", "a200", "a201", "a202", "a203", "a204", "a205", "a206", "a207", "a208", "a209", "a210", "a211", "a212", "a213", "a214", \
  "a215", "a216", "a217", "a218", "a219", "a220", "a221", "a222", "a223", "a224", "a225", "a226", "a227", "a228", "a229", "a230", "a231", "a232", "a233", "a234", \
  "a235", "a236", "a237", "a238", "a239", "a240", "a241", "a242", "a243", "a244", "a245", "a246", "a247", "a248", "a249", "a250", "a251", "a252", "a253", "a254", \
  "a255", "s8", "s9", "s10", "s11", "s12", "s13", "s14", "s15", "s16", "s17", "s18", "s19", "s20", "s21", "s22", "s23", "s24", "s25", "s26", \
  "s27", "s28", "s29", "s30", "s31", "s36", "s37", "s38", "s39", "s40", "s41", "s42", "s43", "s44", "s45", "s46", "s47", "s48", "s49", "s50", \
  "s51", "s52", "s53", "s54", "s55", "s56", "s57", "s58", "s59", "s60", "s61", "s63", "s64", "s65", "s66", "s67", "s68", "s69", "s70", "s71", \
  "s88", "s89", "s90", "s91", "s92", "s93", "s94", "s95", "s96", "vcc", "scc", "memory"
#define VALUE_ASM \
  "s_mov_b32 s40, %18\n" \
  "s_add_u32 s44, %14, 0\n" \
  "buffer_load_dwordx4 a[0:3], %7, %13, s44 offen\n" \
  "buffer_load_dwordx4 a[4:7], %7, %13, s44 offen offset:1024\n" \
  "s_add_u32 s44, %14, 2048\n" \
  "buffer_load_dwordx4 a[8:11], %7, %13, s44 offen\n" \
  "buffer_load_dwordx4 a[12:15], %7, %13, s44 offen offset:1024\n" \
  "s_add_u32 s44, %14, 4096\n" \
  "buffer_load_dwordx4 a[16:19], %7, %13, s44 offen\n" \
  "buffer_load_dwordx4 a[20:23], %7, %13, s44 offen offset:1024\n" \
  "s_add_u32 s44, %14, 6144\n" \
  "buffer_load_dwordx4 a[24:27], %7, %13, s44 offen\n" \
  "buffer_load_dwordx4 a[28:31], %7, %13, s44 offen offset:1024\n" \
  "s_add_u32 s44, %14, 8192\n" \
  "buffer_load_dwordx4 a[32:35], %7, %13, s44 offen\n" \
  "buffer_load_dwordx4 a[36:39], %7, %13, s44 offen offset:1024\n" \
  "s_add_u32 s44, %14, 10240\n" \
  "buffer_load_dwordx4 a[40:43], %7, %13, s44 offen\n" \
  "buffer_load_dwordx4 a[44:47], %7, %13, s44 offen offset:1024\n" \
  "s_add_u32 s44, %14, 12288\n" \
  "buffer_load_dwordx4 a[48:51], %7, %13, s44 offen\n" \
  "buffer_load_dwordx4 a[52:55], %7, %13, s44 offen offset:1024\n" \
  "s_add_u32 s44, %14, 28672\n" \
  "buffer_load_dwordx4 a[56:59], %7, %13, s44 offen\n" \
  "buffer_load_dwordx4 a[60:63], %7, %13, s44 offen offset:1024\n" \
  "s_add_u32 s44, %14, 30720\n" \
  "buffer_load_dwordx4 a[64:67], %7, %13, s44 offen\n" \
  "buffer_load_dwordx4 a[68:71], %7, %13, s44 offen offset:1024\n" \
  "s_add_u32 s44, %14, 32768\n" \
  "buffer_load_dwordx4 a[72:75], %7, %13, s44 offen\n" \
  "buffer_load_dwordx4 a[76:79], %7, %13, s44 offen offset:1024\n" \
  "s_add_u32 s44, %14, 34816\n" \
  "buffer_load_dwordx4 a[80:83], %7, %13, s44 offen\n" \
  "buffer_load_dwordx4 a[84:87], %7, %13, s44 offen offset:1024\n" \
  "s_add_u32 s44, %14, 36864\n" \
  "buffer_load_dwordx4 a[88:91], %7, %13, s44 offen\n" \
  "buffer_load_dwordx4 a[92:95], %7, %13, s44 offen offset:1024\n" \
  "s_add_u32 s44, %14, 38912\n" \
  "buffer_load_dwordx4 a[96:99], %7, %13, s44 offen\n" \
  "buffer_load_dwordx4 a[100:103], %7, %13, s44 offen offset:1024\n" \
  "s_add_u32 s44, %14, 40960\n" \
  "buffer_load_dwordx4 a[104:107], %7, %13, s44 offen\n" \
  "buffer_load_dwordx4 a[108:111], %7, %13, s44 offen offset:1024\n" \
  "s_add_u32 s44, %14, 57344\n" \
  "buffer_load_dwordx4 a[112:115], %7, %13, s44 offen\n" \
  "buffer_load_dwordx4 a[116:119], %7, %13, s44 offen offset:1024\n" \
  "s_add_u32 s44, %14, 59392\n" \
  "buffer_load_dwordx4 a[120:123], %7, %13, s44 offen\n" \
  "buffer_load_dwordx4 a[124:127], %7, %13, s44 offen offset:1024\n" \
  "s_add_u32 s44, %14, 61440\n" \
  "buffer_load_dwordx4 a[128:131], %7, %13, s44 offen\n" \
  "buffer_load_dwordx4 a[132:135], %7, %13, s44 offen offset:1024\n" \
  "s_add_u32 s44, %14, 63488\n" \
  "buffer_load_dwordx4 a[136:139], %7, %13, s44 offen\n" \
  "buffer_load_dwordx4 a[140:143], %7, %13, s44 offen offset:1024\n" \
  "s_add_u32 s44, %14, 65536\n" \
  "buffer_load_dwordx4 a[144:147], %7, %13, s44 offen\n" \
  "buffer_load_dwordx4 a[148:151], %7, %13, s44 offen offset:1024\n" \
  "s_add_u32 s44, %14, 67584\n" \
  "buffer_load_dwordx4 a[152:155], %7, %13, s44 offen\n" \
  "buffer_load_dwordx4 a[156:159], %7, %13, s44 offen offset:1024\n" \
  "s_add_u32 s44, %14, 69632\n" \
  "buffer_load_dwordx4 a[160:163], %7, %13, s44 offen\n" \
  "buffer_load_dwordx4 a[164:167], %7, %13, s44 offen offset:1024\n" \
  "s_add_u32 s44, %14, 86016\n" \
  "buffer_load_dwordx4 a[168:171], %7, %13, s44 offen\n" \
  "buffer_load_dwordx4 a[172:175], %7, %13, s44 offen offset:1024\n" \
  "s_add_u32 s44, %14, 88064\n" \
  "buffer_load_dwordx4 a[176:179], %7, %13, s44 offen\n" \
  "buffer_load_dwordx4 a[180:183], %7, %13, s44 offen offset:1024\n" \
  "s_add_u32 s44, %14, 90112\n" \
  "buffer_load_dwordx4 a[184:187], %7, %13, s44 offen\n" \
  "buffer_load_dwordx4 a[188:191], %7, %13, s44 offen offset:1024\n" \
  "s_add_u32 s44, %14, 92160\n" \
  "buffer_load_dwordx4 a[192:195], %7, %13, s44 offen\n" \
  "buffer_load_dwordx4 a[196:199], %7, %13, s44 offen offset:1024\n" \
  "s_add_u32 s44, %14, 94208\n" \
  "buffer_load_dwordx4 a[200:203], %7, %13, s44 offen\n" \
  "buffer_load_dwordx4 a[204:207], %7, %13, s44 offen offset:1024\n" \
  "s_add_u32 s44, %14, 96256\n" \
  "buffer_load_dwordx4 a[208:211], %7, %13, s44 offen\n" \
  "buffer_load_dwordx4 a[212:215], %7, %13, s44 offen offset:1024\n" \
  "s_add_u32 s44, %14, 98304\n" \
  "buffer_load_dwordx4 a[216:219], %7, %13, s44 offen\n" \
  "buffer_load_dwordx4 a[220:223], %7, %13, s44 offen offset:1024\n" \
  "s_getpc_b64 s[88:89]\n" \
  "L_pf_%=:\n" \
  "v_lshlrev_b32 v10, 3, %7\n" \
  "v_min_u32 v10, L_end_%=-L_pf_%=, v10\n" \
  "global_load_dword v62, v10, s[88:89]\n" \
  "v_lshrrev_b32 v11, 2, %7\n" \
  "v_min_u32 v11, 56, v11\n" \
  "s_mov_b32 s41, %c23\n" \
  "buffer_load_dword v10, v11, %13, s41 offen sc1\n" \
  "s_waitcnt vmcnt(0) lgkmcnt(0)\n" \
  "s_barrier\n" \
  "L_cfg_%=:\n" \
  "v_cmp_eq_u32 vcc, -1, v10\n" \
  "s_cbranch_vccz L_cfgok_%=\n" \
  "buffer_load_dword v10, v11, %13, s41 offen sc1\n" \
  "s_waitcnt vmcnt(0)\n" \
  "s_sub_u32 s40, s40, 1\n" \
  "s_cbranch_scc0 L_cfg_%=\n" \
  "s_mov_b32 s40, 0\n" \
  "v_mov_b32 v10, 0x1000\n" \
  "L_cfgok_%=:\n" \
  "s_nop 1\n" \
  "s_mov_b32 s38, 8000\n" \
  "s_memtime s[88:89]\n" \
  "s_memrealtime s[8:9]\n" \
  "s_nop 0\n" \
  "s_memrealtime s[10:11]\n" \
  "s_nop 0\n" \
  "s_memrealtime s[12:13]\n" \
  "s_nop 0\n" \
  "s_memrealtime s[14:15]\n" \
  "s_nop 0\n" \
  "s_memrealtime s[16:17]\n" \
  "s_nop 0\n" \
  "s_memrealtime s[18:19]\n" \
  "s_nop 0\n" \
  "s_memrealtime s[20:21]\n" \
  "s_nop 0\n" \
  "s_memrealtime s[22:23]\n" \
  "s_nop 0\n" \
  "s_memrealtime s[24:25]\n" \
  "s_nop 0\n" \
  "s_memrealtime s[26:27]\n" \
  "s_nop 0\n" \
  "s_memrealtime s[28:29]\n" \
  "s_nop 0\n" \
  "s_memrealtime s[30:31]\n" \
  "s_waitcnt lgkmcnt(0)\n" \
  "s_add_u32 s8, s8, s10\n" \
  "s_add_u32 s8, s8, s12\n" \
  "s_add_u32 s8, s8, s14\n" \
  "s_add_u32 s8, s8, s16\n" \
  "s_add_u32 s8, s8, s18\n" \
  "s_add_u32 s8, s8, s20\n" \
  "s_add_u32 s8, s8, s22\n" \
  "s_add_u32 s8, s8, s24\n" \
  "s_add_u32 s8, s8, s26\n" \
  "s_add_u32 s8, s8, s28\n" \
  "s_add_u32 s8, s8, s30\n" \
  "s_mov_b32 s36, s8\n" \
  "s_mov_b32 s37, s88\n" \
  "v_lshrrev_b32 v15, 1, %7\n" \
  "s_lshr_b32 s47, %15, 1\n" \
  "s_and_b32 s48, %15, 1\n" \
  "s_lshl_b32 s48, s48, 11\n" \
  "s_add_u32 s42, s47, 3\n" \
  "s_nop 0\n" \
  "v_readlane_b32 s50, v10, s42\n" \
  "v_readlane_b32 s53, v10, 0\n" \
  "v_readlane_b32 s90, v10, 1\n" \
  "v_readlane_b32 s56, v10, 2\n" \
  "s_add_u32 s42, %16, 3\n" \
  "s_nop 0\n" \
  "v_readlane_b32 s61, v10, s42\n" \
  "s_nop 3\n" \
  "s_add_u32 s50, s50, s48\n" \
  "s_add_u32 s61, s61, %17\n" \
  "s_add_u32 s42, s47, 8\n" \
  "s_nop 0\n" \
  "v_readlane_b32 s51, v10, s42\n" \
  "v_readlane_b32 s54, v10, 5\n" \
  "v_readlane_b32 s91, v10, 6\n" \
  "v_readlane_b32 s57, v10, 7\n" \
  "s_add_u32 s42, %16, 8\n" \
  "s_nop 0\n" \
  "v_readlane_b32 s62, v10, s42\n" \
  "s_nop 3\n" \
  "s_add_u32 s51, s51, s48\n" \
  "s_add_u32 s62, s62, %17\n" \
  "s_add_u32 s42, s47, 13\n" \
  "s_nop 0\n" \
  "v_readlane_b32 s52, v10, s42\n" \
  "v_readlane_b32 s55, v10, 10\n" \
  "v_readlane_b32 s92, v10, 11\n" \
  "v_readlane_b32 s58, v10, 12\n" \
  "s_add_u32 s42, %16, 13\n" \
  "s_nop 0\n" \
  "v_readlane_b32 s63, v10, s42\n" \
  "s_nop 3\n" \
  "s_add_u32 s52, s52, s48\n" \
  "s_add_u32 s63, s63, %17\n" \
  "s_sub_u32 s49, %1, %0\n" \
  "s_mov_b32 s95, 0\n" \
  "s_mov_b32 s93, 0\n" \
  "s_mov_b32 s94, 0\n" \
  "s_mov_b32 s46, 1\n" \
  "s_mov_b32 s47, 0\n" \
  "s_mov_b32 s48, %c23+128\n" \
  "v_mov_b32 v14, -1\n" \
  "s_cmp_eq_u32 %15, 0\n" \
  "s_cbranch_scc0 L_nwm_%=\n" \
  "buffer_store_dword v14, %10, %13, s61 offen sc1\n" \
  "buffer_store_dword v14, %10, %13, s62 offen sc1\n" \
  "buffer_store_dword v14, %10, %13, s63 offen sc1\n" \
  "L_nwm_%=:\n" \
  "v_and_b32 v11, 16, %7\n" \
  "v_mul_u32_u24 v11, 112, v11\n" \
  "L_f0_%=:\n" \
  "buffer_load_dword v9, v11, %13, %3 offen sc1\n" \
  "s_waitcnt vmcnt(0)\n" \
  "v_cmp_ne_u32 vcc, 1, v9\n" \
  "s_cbranch_vccz L_f0ok_%=\n" \
  "s_sub_u32 s40, s40, 1\n" \
  "s_cbranch_scc0 L_f0_%=\n" \
  "s_mov_b32 s40, 0\n" \
  "L_f0ok_%=:\n" \
  "s_mov_b32 s43, 0\n" \
  "v_mov_b32 v14, -1\n" \
  "v_mov_b32 v13, -1\n" \
  "v_mov_b32 v73, 0\n" \
  "v_mov_b64 v[214:215], 0\n" \
  "v_mov_b32 v75, 0\n" \
  "v_mov_b64 v[218:219], 0\n" \
  "v_mov_b32 v77, 0\n" \
  "v_mov_b64 v[222:223], 0\n" \
  "v_mov_b32 v79, 0\n" \
  "v_mov_b64 v[226:227], 0\n" \
  "v_mov_b64 v[52:53], 0\n" \
  "v_mov_b64 v[54:55], 0\n" \
  "v_mov_b64 v[56:57], 0\n" \
  "v_mov_b64 v[58:59], 0\n" \
  "v_mov_b64 v[60:61], 0\n" \
  "v_mov_b64 v[62:63], 0\n" \
  "v_mov_b64 v[64:65], 0\n" \
  "v_mov_b64 v[66:67], 0\n" \
  "s_branch L_first_%=\n" \
  "L_step_%=:\n" \
  "L_w0a_%=:\n" \
  "s_sub_u32 s39, s64, s36\n" \
  "s_add_u32 s64, s64, s41\n" \
  "s_add_u32 s46, s46, 1\n" \
  "s_cmp_gt_i32 s39, 0x20000\n" \
  "s_cbranch_scc1 L_w2a_%=\n" \
  "s_mul_i32 s39, s39, s38\n" \
  "s_ashr_i32 s39, s39, 12\n" \
  "s_add_u32 s39, s39, s37\n" \
  "L_w1a_%=:\n" \
  "s_memtime s[88:89]\n" \
  "s_waitcnt lgkmcnt(0)\n" \
  "s_sub_u32 s42, s39, s88\n" \
  "s_cmp_gt_i32 s42, 128\n" \
  "s_cbranch_scc1 L_w1a_%=\n" \
  "s_cmp_lt_i32 s42, -400\n" \
  "s_cbranch_scc1 L_w0a_%=\n" \
  "s_max_i32 s42, s42, 0\n" \
  "s_getpc_b64 s[88:89]\n" \
  "L_pca_%=:\n" \
  "s_andn2_b32 s42, s42, 3\n" \
  "s_sub_u32 s42, L_w2a_%=-L_pca_%=, s42\n" \
  "s_add_u32 s88, s88, s42\n" \
  "s_addc_u32 s89, s89, 0\n" \
  "s_setpc_b64 s[88:89]\n" \
  ".p2align 5\n" \
  "s_nop 0\n" \
  "s_nop 0\n" \
  "s_nop 0\n" \
  "s_nop 0\n" \
  "s_nop 0\n" \
  "s_nop 0\n" \
  "s_nop 0\n" \
  "s_nop 0\n" \
  "s_nop 0\n" \
  "s_nop 0\n" \
  "s_nop 0\n" \
  "s_nop 0\n" \
  "s_nop 0\n" \
  "s_nop 0\n" \
  "s_nop 0\n" \
  "s_nop 0\n" \
  "s_nop 0\n" \
  "s_nop 0\n" \
  "s_nop 0\n" \
  "s_nop 0\n" \
  "s_nop 0\n" \
  "s_nop 0\n" \
  "s_nop 0\n" \
  "s_nop 0\n" \
  "s_nop 0\n" \
  "s_nop 0\n" \
  "s_nop 0\n" \
  "s_nop 0\n" \
  "s_nop 0\n" \
  "s_nop 0\n" \
  "s_nop 0\n" \
  "s_nop 0\n" \
  "L_w2a_%=:\n" \
  "L_first_%=:\n" \
  "buffer_load_dwordx4 v[44:47], %7, %13, %0 offen sc1\n" \
  "buffer_load_dwordx4 v[48:51], %7, %13, %1 offen sc1\n" \
  "s_cmp_eq_u32 s43, 0\n" \
  "s_cbranch_scc1 L_nh_%=\n" \
  "s_cmp_eq_u32 s43, 1\n" \
  "s_cselect_b32 s42, 100, 0\n" \
  "s_add_u32 s65, s65, s42\n" \
  "v_readfirstlane_b32 s44, v13\n" \
  "s_nop 7\n" \
  "s_and_b32 s89, s44, 1023\n" \
  "s_sub_u32 s42, s46, s89\n" \
  "s_cbranch_scc1 L_noapa_%=\n" \
  "s_cmp_lt_u32 s47, s89\n" \
  "s_cbranch_scc1 L_noapa_%=\n" \
  "s_cmp_le_u32 s89, s93\n" \
  "s_cbranch_scc1 L_noapa_%=\n" \
  "s_cmp_eq_u32 s44, -1\n" \
  "s_cbranch_scc1 L_noapa_%=\n" \
  "s_mov_b32 s93, s89\n" \
  "s_bfe_u32 s88, s44, 0xb000a\n" \
  "s_sub_u32 s88, s88, 512\n" \
  "s_mul_i32 s42, s42, s88\n" \
  "s_add_u32 s64, s64, s42\n" \
  "s_sub_u32 s42, s47, s89\n" \
  "s_mul_i32 s42, s42, s88\n" \
  "s_add_u32 s65, s65, s42\n" \
  "s_lshr_b32 s44, s44, 21\n" \
  "s_add_u32 s65, s65, s44\n" \
  "s_sub_u32 s65, s65, 512\n" \
  "s_add_u32 s41, s41, s88\n" \
  "s_add_u32 s48, s48, 4\n" \
  "L_noapa_%=:\n" \
  "L_nh_%=:\n" \
  "s_cmp_eq_u32 %15, 0\n" \
  "s_cbranch_scc0 L_nv_%=\n" \
  "s_cmp_eq_u32 s43, 63\n" \
  "s_cbranch_scc1 L_nv_%=\n" \
  "L_flag_%=:\n" \
  "v_cmp_ne_u32 vcc, 1, v9\n" \
  "s_cbranch_vccz L_flagok_%=\n" \
  "buffer_load_dword v9, off, %13, %3 sc1\n" \
  "s_waitcnt vmcnt(0)\n" \
  "s_sub_u32 s40, s40, 1\n" \
  "s_cbranch_scc0 L_flag_%=\n" \
  "s_mov_b32 s40, 0\n" \
  "L_flagok_%=:\n" \
  "s_add_u32 s44, %2, 0\n" \
  "buffer_load_dwordx4 v[84:87], %7, %13, s44 offen offset:0 sc1\n" \
  "buffer_load_dwordx4 v[88:91], %7, %13, s44 offen offset:2048 sc1\n" \
  "s_add_u32 s44, %2, 4096\n" \
  "buffer_load_dwordx4 v[92:95], %7, %13, s44 offen offset:0 sc1\n" \
  "buffer_load_dwordx4 v[96:99], %7, %13, s44 offen offset:2048 sc1\n" \
  "s_add_u32 s44, %2, 8192\n" \
  "buffer_load_dwordx4 v[100:103], %7, %13, s44 offen offset:0 sc1\n" \
  "buffer_load_dwordx4 v[104:107], %7, %13, s44 offen offset:2048 sc1\n" \
  "s_add_u32 s44, %2, 12288\n" \
  "buffer_load_dwordx4 v[108:111], %7, %13, s44 offen offset:0 sc1\n" \
  "buffer_load_dwordx4 v[112:115], %7, %13, s44 offen offset:2048 sc1\n" \
  "s_add_u32 s44, %2, 16384\n" \
  "buffer_load_dwordx4 v[116:119], %7, %13, s44 offen offset:0 sc1\n" \
  "buffer_load_dwordx4 v[120:123], %7, %13, s44 offen offset:2048 sc1\n" \
  "s_add_u32 s44, %2, 20480\n" \
  "buffer_load_dwordx4 v[124:127], %7, %13, s44 offen offset:0 sc1\n" \
  "buffer_load_dwordx4 v[128:131], %7, %13, s44 offen offset:2048 sc1\n" \
  "s_add_u32 s44, %2, 24576\n" \
  "buffer_load_dwordx4 v[132:135], %7, %13, s44 offen offset:0 sc1\n" \
  "buffer_load_dwordx4 v[136:139], %7, %13, s44 offen offset:2048 sc1\n" \
  "s_add_u32 s44, %2, 28672\n" \
  "buffer_load_dwordx4 v[140:143], %7, %13, s44 offen offset:0 sc1\n" \
  "buffer_load_dwordx4 v[144:147], %7, %13, s44 offen offset:2048 sc1\n" \
  "s_add_u32 %3, %3, 1792\n" \
  "s_add_u32 %2, %2, 458752\n" \
  "buffer_load_dword v9, off, %13, %3 sc1\n" \
  "buffer_load_dword v13, off, %13, s48 sc1\n" \
  "s_waitcnt vmcnt(19)\n" \
  "ds_write_b128 %8, v[44:47]\n" \
  "v_max_u32 v10, v44, v45\n" \
  "v_max3_u32 v10, v10, v46, v47\n" \
  "s_waitcnt vmcnt(18)\n" \
  "ds_write_b128 %8, v[48:51] offset:1024\n" \
  "v_max3_u32 v10, v10, v48, v49\n" \
  "v_max3_u32 v10, v10, v50, v51\n" \
  "v_cmp_eq_u32 vcc, -1, v10\n" \
  "s_cbranch_vccz L_go_%=\n" \
  "s_branch L_poll_%=\n" \
  "L_nv_%=:\n" \
  "buffer_load_dword v13, off, %13, s48 sc1\n" \
  "s_waitcnt vmcnt(2)\n" \
  "ds_write_b128 %8, v[44:47]\n" \
  "v_max_u32 v10, v44, v45\n" \
  "v_max3_u32 v10, v10, v46, v47\n" \
  "s_waitcnt vmcnt(1)\n" \
  "ds_write_b128 %8, v[48:51] offset:1024\n" \
  "v_max3_u32 v10, v10, v48, v49\n" \
  "v_max3_u32 v10, v10, v50, v51\n" \
  "v_cmp_eq_u32 vcc, -1, v10\n" \
  "s_cbranch_vccz L_go_%=\n" \
  "L_poll_%=:\n" \
  "s_sub_u32 s40, s40, 1\n" \
  "s_cbranch_scc1 L_pto_%=\n" \
  "s_memtime s[88:89]\n" \
  "s_memrealtime s[8:9]\n" \
  "s_nop 0\n" \
  "s_memrealtime s[10:11]\n" \
  "s_nop 0\n" \
  "s_memrealtime s[12:13]\n" \
  "s_nop 0\n" \
  "s_memrealtime s[14:15]\n" \
  "s_nop 0\n" \
  "s_memrealtime s[16:17]\n" \
  "s_nop 0\n" \
  "s_memrealtime s[18:19]\n" \
  "s_nop 0\n" \
  "s_memrealtime s[20:21]\n" \
  "s_nop 0\n" \
  "s_memrealtime s[22:23]\n" \
  "s_nop 0\n" \
  "s_memrealtime s[24:25]\n" \
  "s_nop 0\n" \
  "s_memrealtime s[26:27]\n" \
  "s_nop 0\n" \
  "s_memrealtime s[28:29]\n" \
  "s_nop 0\n" \
  "s_memrealtime s[30:31]\n" \
  "s_waitcnt lgkmcnt(0)\n" \
  "s_add_u32 s8, s8, s10\n" \
  "s_add_u32 s8, s8, s12\n" \
  "s_add_u32 s8, s8, s14\n" \
  "s_add_u32 s8, s8, s16\n" \
  "s_add_u32 s8, s8, s18\n" \
  "s_add_u32 s8, s8, s20\n" \
  "s_add_u32 s8, s8, s22\n" \
  "s_add_u32 s8, s8, s24\n" \
  "s_add_u32 s8, s8, s26\n" \
  "s_add_u32 s8, s8, s28\n" \
  "s_add_u32 s8, s8, s30\n" \
  "s_mov_b32 s36, s8\n" \
  "s_mov_b32 s37, s88\n" \
  "L_w0b_%=:\n" \
  "s_sub_u32 s39, s64, s36\n" \
  "s_add_u32 s64, s64, s41\n" \
  "s_add_u32 s46, s46, 1\n" \
  "s_cmp_gt_i32 s39, 0x20000\n" \
  "s_cbranch_scc1 L_w2b_%=\n" \
  "s_mul_i32 s39, s39, s38\n" \
  "s_ashr_i32 s39, s39, 12\n" \
  "s_add_u32 s39, s39, s37\n" \
  "L_w1b_%=:\n" \
  "s_memtime s[88:89]\n" \
  "s_waitcnt lgkmcnt(0)\n" \
  "s_sub_u32 s42, s39, s88\n" \
  "s_cmp_gt_i32 s42, 128\n" \
  "s_cbranch_scc1 L_w1b_%=\n" \
  "s_cmp_lt_i32 s42, -400\n" \
  "s_cbranch_scc1 L_w0b_%=\n" \
  "s_max_i32 s42, s42, 0\n" \
  "s_getpc_b64 s[88:89]\n" \
  "L_pcb_%=:\n" \
  "s_andn2_b32 s42, s42, 3\n" \
  "s_sub_u32 s42, L_w2b_%=-L_pcb_%=, s42\n" \
  "s_add_u32 s88, s88, s42\n" \
  "s_addc_u32 s89, s89, 0\n" \
  "s_setpc_b64 s[88:89]\n" \
  ".p2align 5\n" \
  "s_nop 0\n" \
  "s_nop 0\n" \
  "s_nop 0\n" \
  "s_nop 0\n" \
  "s_nop 0\n" \
  "s_nop 0\n" \
  "s_nop 0\n" \
  "s_nop 0\n" \
  "s_nop 0\n" \
  "s_nop 0\n" \
  "s_nop 0\n" \
  "s_nop 0\n" \
  "s_nop 0\n" \
  "s_nop 0\n" \
  "s_nop 0\n" \
  "s_nop 0\n" \
  "s_nop 0\n" \
  "s_nop 0\n" \
  "s_nop 0\n" \
  "s_nop 0\n" \
  "s_nop 0\n" \
  "s_nop 0\n" \
  "s_nop 0\n" \
  "s_nop 0\n" \
  "s_nop 0\n" \
  "s_nop 0\n" \
  "s_nop 0\n" \
  "s_nop 0\n" \
  "s_nop 0\n" \
  "s_nop 0\n" \
  "s_nop 0\n" \
  "s_nop 0\n" \
  "L_w2b_%=:\n" \
  "buffer_load_dwordx4 v[44:47], %7, %13, %0 offen sc1\n" \
  "buffer_load_dwordx4 v[48:51], %7, %13, %1 offen sc1\n" \
  "s_waitcnt vmcnt(1)\n" \
  "ds_write_b128 %8, v[44:47]\n" \
  "v_max_u32 v10, v44, v45\n" \
  "v_max3_u32 v10, v10, v46, v47\n" \
  "s_waitcnt vmcnt(0)\n" \
  "ds_write_b128 %8, v[48:51] offset:1024\n" \
  "v_max3_u32 v10, v10, v48, v49\n" \
  "v_max3_u32 v10, v10, v50, v51\n" \
  "v_cmp_eq_u32 vcc, -1, v10\n" \
  "s_cbranch_vccz L_go_%=\n" \
  "s_branch L_poll_%=\n" \
  "L_pto_%=:\n" \
  "s_mov_b32 s40, 0\n" \
  "L_go_%=:\n" \
  "s_waitcnt lgkmcnt(0)\n" \
  "s_barrier\n" \
  "ds_read_b128 v[16:19], %7 offset:0\n" \
  "ds_read_b128 v[20:23], %7 offset:1024\n" \
  "ds_read_b128 v[24:27], %7 offset:2048\n" \
  "ds_read_b128 v[28:31], %7 offset:3072\n" \
  "ds_read_b128 v[32:35], %7 offset:4096\n" \
  "ds_read_b128 v[36:39], %7 offset:5120\n" \
  "ds_read_b128 v[40:43], %7 offset:6144\n" \
  "s_mov_b32 %0, s51\n" \
  "s_add_u32 %1, %0, s49\n" \
  "v_mov_b64 v[80:81], 0\n" \
  "v_mov_b64 v[82:83], 0\n" \
  "s_cmp_eq_u32 %15, 0\n" \
  "s_cbranch_scc1 L_wn_%=\n" \
  "s_cmp_eq_u32 s95, 1\n" \
  "s_cbranch_scc0 L_cp1_%=\n" \
  "s_cmp_eq_u32 s43, 63\n" \
  "s_cbranch_scc1 L_cp1_%=\n" \
  "s_lshl_b32 s44, %17, 2\n" \
  "v_add_u32 v10, s44, %7\n" \
  "ds_read_b128 v[44:47], v10\n" \
  "s_waitcnt lgkmcnt(0)\n" \
  "buffer_store_dwordx4 v[44:47], %7, %13, %4 offen sc1\n" \
  "L_cp1_%=:\n" \
  "s_mov_b32 s95, %20\n" \
  "s_add_u32 %4, %4, 7168\n" \
  "s_cmp_eq_u32 s43, 0\n" \
  "s_cbranch_scc1 L_nf_%=\n" \
  "s_cmp_eq_u32 s43, 63\n" \
  "s_cselect_b32 %5, %22, %5\n" \
  "buffer_store_dwordx4 v[68:71], %7, %13, %5 offen sc1\n" \
  "s_sub_u32 s44, %5, 1024\n" \
  "s_add_u32 %5, %5, 114688\n" \
  "s_cmp_eq_u32 %15, 1\n" \
  "s_cbranch_scc0 L_nf_%=\n" \
  "buffer_store_dwordx4 v[84:87], %7, %13, s44 offen sc1\n" \
  "s_cmp_eq_u32 s43, 1\n" \
  "s_cbranch_scc1 L_nf_%=\n" \
  "v_mov_b32 v10, 1\n" \
  "s_mov_b64 exec, 1\n" \
  "buffer_store_dword v10, off, %13, %6 sc1\n" \
  "s_mov_b64 exec, -1\n" \
  "s_add_u32 %6, %6, 1792\n" \
  "L_nf_%=:\n" \
  "s_cmp_eq_u32 %15, 3\n" \
  "s_cbranch_scc0 L_wn_%=\n" \
  "buffer_store_dword v14, %10, %13, s63 offen sc1\n" \
  "L_wn_%=:\n" \
  "s_waitcnt lgkmcnt(6)\n" \
  "v_smfmac_f32_16x16x64_bf16 v[52:55], v[16:19], a[0:7], %11\n" \
  "v_smfmac_f32_16x16x64_bf16 v[56:59], v[16:19], a[56:63], %11\n" \
  "v_smfmac_f32_16x16x64_bf16 v[60:63], v[16:19], a[112:119], %11\n" \
  "v_smfmac_f32_16x16x64_bf16 v[64:67], v[16:19], a[168:175], %11\n" \
  "s_waitcnt lgkmcnt(5)\n" \
  "v_smfmac_f32_16x16x64_bf16 v[52:55], v[20:23], a[8:15], %11\n" \
  "v_smfmac_f32_16x16x64_bf16 v[56:59], v[20:23], a[64:71], %11\n" \
  "v_smfmac_f32_16x16x64_bf16 v[60:63], v[20:23], a[120:127], %11\n" \
  "v_smfmac_f32_16x16x64_bf16 v[64:67], v[20:23], a[176:183], %11\n" \
  "s_waitcnt lgkmcnt(4)\n" \
  "v_smfmac_f32_16x16x64_bf16 v[52:55], v[24:27], a[16:23], %11\n" \
  "v_smfmac_f32_16x16x64_bf16 v[56:59], v[24:27], a[72:79], %11\n" \
  "v_smfmac_f32_16x16x64_bf16 v[60:63], v[24:27], a[128:135], %11\n" \
  "v_smfmac_f32_16x16x64_bf16 v[64:67], v[24:27], a[184:191], %11\n" \
  "s_waitcnt lgkmcnt(3)\n" \
  "v_smfmac_f32_16x16x64_bf16 v[52:55], v[28:31], a[24:31], %11\n" \
  "v_smfmac_f32_16x16x64_bf16 v[56:59], v[28:31], a[80:87], %11\n" \
  "v_smfmac_f32_16x16x64_bf16 v[60:63], v[28:31], a[136:143], %11\n" \
  "v_smfmac_f32_16x16x64_bf16 v[64:67], v[28:31], a[192:199], %11\n" \
  "s_waitcnt lgkmcnt(2)\n" \
  "v_smfmac_f32_16x16x64_bf16 v[52:55], v[32:35], a[32:39], %11\n" \
  "v_smfmac_f32_16x16x64_bf16 v[56:59], v[32:35], a[88:95], %11\n" \
  "v_smfmac_f32_16x16x64_bf16 v[60:63], v[32:35], a[144:151], %11\n" \
  "v_smfmac_f32_16x16x64_bf16 v[64:67], v[32:35], a[200:207], %11\n" \
  "s_waitcnt lgkmcnt(1)\n" \
  "v_smfmac_f32_16x16x64_bf16 v[52:55], v[36:39], a[40:47], %11\n" \
  "v_smfmac_f32_16x16x64_bf16 v[56:59], v[36:39], a[96:103], %11\n" \
  "v_smfmac_f32_16x16x64_bf16 v[60:63], v[36:39], a[152:159], %11\n" \
  "v_smfmac_f32_16x16x64_bf16 v[64:67], v[36:39], a[208:215], %11\n" \
  "s_waitcnt lgkmcnt(0)\n" \
  "v_smfmac_f32_16x16x64_bf16 v[52:55], v[40:43], a[48:55], %11\n" \
  "v_smfmac_f32_16x16x64_bf16 v[56:59], v[40:43], a[104:111], %11\n" \
  "v_smfmac_f32_16x16x64_bf16 v[60:63], v[40:43], a[160:167], %11\n" \
  "v_smfmac_f32_16x16x64_bf16 v[64:67], v[40:43], a[216:223], %11\n" \
  "s_nop 7\n" \
  "v_pk_add_f32 v[52:53], v[52:53], v[54:55]\n" \
  "v_pk_add_f32 v[56:57], v[56:57], v[58:59]\n" \
  "v_pk_add_f32 v[60:61], v[60:61], v[62:63]\n" \
  "v_pk_add_f32 v[64:65], v[64:65], v[66:67]\n" \
  "v_cvt_pk_bf16_f32 v72, v52, v53\n" \
  "v_cvt_pk_bf16_f32 v74, v56, v57\n" \
  "v_cvt_pk_bf16_f32 v76, v60, v61\n" \
  "v_cvt_pk_bf16_f32 v78, v64, v65\n" \
  "v_min_u32 v68, -2, v72\n" \
  "v_min_u32 v69, -2, v74\n" \
  "v_min_u32 v70, -2, v76\n" \
  "v_min_u32 v71, -2, v78\n" \
  "s_cmp_eq_u32 s43, 63\n" \
  "s_cbranch_scc1 L_last_%=\n" \
  "s_cmp_eq_u32 s43, 0\n" \
  "s_cbranch_scc0 L_na_%=\n" \
  "s_mov_b32 s44, %c23+384\n" \
  "L_an_%=:\n" \
  "buffer_load_dwordx4 v[48:51], off, %13, s44 sc1\n" \
  "s_waitcnt vmcnt(0)\n" \
  "v_readfirstlane_b32 s64, v48\n" \
  "v_readfirstlane_b32 s41, v49\n" \
  "v_readfirstlane_b32 s45, v50\n" \
  "v_readfirstlane_b32 s38, v51\n" \
  "s_nop 0\n" \
  "s_max_u32 s42, s64, s41\n" \
  "s_max_u32 s42, s42, s45\n" \
  "s_max_u32 s42, s42, s38\n" \
  "s_cmp_lg_u32 s42, -1\n" \
  "s_cbranch_scc1 L_anok_%=\n" \
  "s_sub_u32 s40, s40, 1\n" \
  "s_cbranch_scc0 L_an_%=\n" \
  "s_mov_b32 s40, 0\n" \
  "s_mov_b32 s41, 2048\n" \
  "s_mov_b32 s45, 1024\n" \
  "s_mov_b32 s38, 8000\n" \
  "L_anok_%=:\n" \
  "s_sub_u32 s42, s64, s36\n" \
  "s_add_u32 s42, s42, 0x20000\n" \
  "s_cmp_lt_u32 s42, 0x40000\n" \
  "s_cselect_b32 s64, s64, s36\n" \
  "s_add_u32 s65, s64, s45\n" \
  "s_sub_u32 s65, s65, 100\n" \
  "s_add_u32 s64, s64, s41\n" \
  "s_add_u32 s64, s64, 60\n" \
  "L_na_%=:\n" \
  "s_add_u32 s43, s43, 1\n" \
  "s_cmp_eq_u32 %15, 0\n" \
  "s_cbranch_scc1 L_comb_%=\n" \
  "v_permlane32_swap_b32 v72, v74\n" \
  "v_permlane32_swap_b32 v76, v78\n" \
  "s_nop 0\n" \
  "v_permlane16_swap_b32 v72, v74\n" \
  "v_permlane16_swap_b32 v76, v78\n" \
  "s_nop 0\n" \
  "v_permlane16_swap_b32 v72, v73\n" \
  "v_permlane16_swap_b32 v74, v75\n" \
  "v_permlane16_swap_b32 v76, v77\n" \
  "v_permlane16_swap_b32 v78, v79\n" \
  "v_mov_b64 v[52:53], 0\n" \
  "v_mov_b64 v[54:55], 0\n" \
  "v_mov_b64 v[56:57], 0\n" \
  "v_mov_b64 v[58:59], 0\n" \
  "v_mov_b64 v[60:61], 0\n" \
  "v_mov_b64 v[62:63], 0\n" \
  "v_mov_b64 v[64:65], 0\n" \
  "v_mov_b64 v[66:67], 0\n" \
  "s_mov_b32 s42, s50\n" \
  "s_mov_b32 s50, s51\n" \
  "s_mov_b32 s51, s52\n" \
  "s_mov_b32 s52, s42\n" \
  "s_mov_b32 s42, s53\n" \
  "s_mov_b32 s53, s54\n" \
  "s_mov_b32 s54, s55\n" \
  "s_mov_b32 s55, s42\n" \
  "s_mov_b32 s42, s56\n" \
  "s_mov_b32 s56, s57\n" \
  "s_mov_b32 s57, s58\n" \
  "s_mov_b32 s58, s42\n" \
  "s_mov_b32 s42, s61\n" \
  "s_mov_b32 s61, s62\n" \
  "s_mov_b32 s62, s63\n" \
  "s_mov_b32 s63, s42\n" \
  "s_mov_b32 s42, s90\n" \
  "s_mov_b32 s90, s91\n" \
  "s_mov_b32 s91, s92\n" \
  "s_mov_b32 s92, s42\n" \
  "ds_write_b128 %8, v[72:75] offset:8192\n" \
  "ds_write_b128 %8, v[76:79] offset:9216\n" \
  "s_mov_b32 s47, s46\n" \
  "s_waitcnt lgkmcnt(0)\n" \
  "s_barrier\n" \
  "ds_read_b128 v[84:87], %7 offset:8192\n" \
  "s_memtime s[88:89]\n" \
  "s_memrealtime s[8:9]\n" \
  "s_nop 0\n" \
  "s_memrealtime s[10:11]\n" \
  "s_nop 0\n" \
  "s_memrealtime s[12:13]\n" \
  "s_nop 0\n" \
  "s_memrealtime s[14:15]\n" \
  "s_nop 0\n" \
  "s_memrealtime s[16:17]\n" \
  "s_nop 0\n" \
  "s_memrealtime s[18:19]\n" \
  "s_nop 0\n" \
  "s_memrealtime s[20:21]\n" \
  "s_nop 0\n" \
  "s_memrealtime s[22:23]\n" \
  "s_nop 0\n" \
  "s_memrealtime s[24:25]\n" \
  "s_nop 0\n" \
  "s_memrealtime s[26:27]\n" \
  "s_nop 0\n" \
  "s_memrealtime s[28:29]\n" \
  "s_nop 0\n" \
  "s_memrealtime s[30:31]\n" \
  "s_waitcnt lgkmcnt(0)\n" \
  "s_add_u32 s8, s8, s10\n" \
  "s_add_u32 s8, s8, s12\n" \
  "s_add_u32 s8, s8, s14\n" \
  "s_add_u32 s8, s8, s16\n" \
  "s_add_u32 s8, s8, s18\n" \
  "s_add_u32 s8, s8, s20\n" \
  "s_add_u32 s8, s8, s22\n" \
  "s_add_u32 s8, s8, s24\n" \
  "s_add_u32 s8, s8, s26\n" \
  "s_add_u32 s8, s8, s28\n" \
  "s_add_u32 s8, s8, s30\n" \
  "s_sub_u32 s42, s8, s36\n" \
  "s_cmp_lt_u32 s42, 2600\n" \
  "s_cbranch_scc0 L_ltn_%=\n" \
  "s_mul_i32 s42, s42, s38\n" \
  "s_lshr_b32 s42, s42, 12\n" \
  "s_sub_u32 s44, s88, s37\n" \
  "s_sub_u32 s42, s44, s42\n" \
  "s_max_i32 s42, s42, -100\n" \
  "s_min_i32 s42, s42, 100\n" \
  "s_add_u32 s38, s38, s42\n" \
  "L_ltn_%=:\n" \
  "s_mov_b32 s36, s8\n" \
  "s_mov_b32 s37, s88\n" \
  "s_cmp_eq_u32 %15, 3\n" \
  "s_cbranch_scc0 L_nd_%=\n" \
  "s_waitcnt vmcnt(0)\n" \
  "L_nd_%=:\n" \
  "s_barrier\n" \
  "s_branch L_step_%=\n" \
  "L_comb_%=:\n" \
  "ds_write_b128 %7, v[68:71] offset:8192\n" \
  "s_memtime s[88:89]\n" \
  "s_memrealtime s[8:9]\n" \
  "s_nop 0\n" \
  "s_memrealtime s[10:11]\n" \
  "s_nop 0\n" \
  "s_memrealtime s[12:13]\n" \
  "s_nop 0\n" \
  "s_memrealtime s[14:15]\n" \
  "s_nop 0\n" \
  "s_memrealtime s[16:17]\n" \
  "s_nop 0\n" \
  "s_memrealtime s[18:19]\n" \
  "s_nop 0\n" \
  "s_memrealtime s[20:21]\n" \
  "s_nop 0\n" \
  "s_memrealtime s[22:23]\n" \
  "s_nop 0\n" \
  "s_memrealtime s[24:25]\n" \
  "s_nop 0\n" \
  "s_memrealtime s[26:27]\n" \
  "s_nop 0\n" \
  "s_memrealtime s[28:29]\n" \
  "s_nop 0\n" \
  "s_memrealtime s[30:31]\n" \
  "s_waitcnt lgkmcnt(0)\n" \
  "s_add_u32 s8, s8, s10\n" \
  "s_add_u32 s8, s8, s12\n" \
  "s_add_u32 s8, s8, s14\n" \
  "s_add_u32 s8, s8, s16\n" \
  "s_add_u32 s8, s8, s18\n" \
  "s_add_u32 s8, s8, s20\n" \
  "s_add_u32 s8, s8, s22\n" \
  "s_add_u32 s8, s8, s24\n" \
  "s_add_u32 s8, s8, s26\n" \
  "s_add_u32 s8, s8, s28\n" \
  "s_add_u32 s8, s8, s30\n" \
  "s_sub_u32 s42, s8, s36\n" \
  "s_cmp_lt_u32 s42, 2600\n" \
  "s_cbranch_scc0 L_ltv_%=\n" \
  "s_mul_i32 s42, s42, s38\n" \
  "s_lshr_b32 s42, s42, 12\n" \
  "s_sub_u32 s44, s88, s37\n" \
  "s_sub_u32 s42, s44, s42\n" \
  "s_max_i32 s42, s42, -100\n" \
  "s_min_i32 s42, s42, 100\n" \
  "s_add_u32 s38, s38, s42\n" \
  "L_ltv_%=:\n" \
  "s_mov_b32 s36, s8\n" \
  "s_mov_b32 s37, s88\n" \
  "L_w0c_%=:\n" \
  "s_sub_u32 s39, s65, s36\n" \
  "s_add_u32 s65, s65, s41\n" \
  "s_add_u32 s47, s47, 1\n" \
  "s_cmp_gt_i32 s39, 0x20000\n" \
  "s_cbranch_scc1 L_w2c_%=\n" \
  "s_mul_i32 s39, s39, s38\n" \
  "s_ashr_i32 s39, s39, 12\n" \
  "s_add_u32 s39, s39, s37\n" \
  "L_w1c_%=:\n" \
  "s_memtime s[88:89]\n" \
  "s_waitcnt lgkmcnt(0)\n" \
  "s_sub_u32 s42, s39, s88\n" \
  "s_cmp_gt_i32 s42, 128\n" \
  "s_cbranch_scc1 L_w1c_%=\n" \
  "s_cmp_lt_i32 s42, -400\n" \
  "s_cbranch_scc1 L_w0c_%=\n" \
  "s_max_i32 s42, s42, 0\n" \
  "s_getpc_b64 s[88:89]\n" \
  "L_pcc_%=:\n" \
  "s_andn2_b32 s42, s42, 3\n" \
  "s_sub_u32 s42, L_w2c_%=-L_pcc_%=, s42\n" \
  "s_add_u32 s88, s88, s42\n" \
  "s_addc_u32 s89, s89, 0\n" \
  "s_setpc_b64 s[88:89]\n" \
  ".p2align 5\n" \
  "s_nop 0\n" \
  "s_nop 0\n" \
  "s_nop 0\n" \
  "s_nop 0\n" \
  "s_nop 0\n" \
  "s_nop 0\n" \
  "s_nop 0\n" \
  "s_nop 0\n" \
  "s_nop 0\n" \
  "s_nop 0\n" \
  "s_nop 0\n" \
  "s_nop 0\n" \
  "s_nop 0\n" \
  "s_nop 0\n" \
  "s_nop 0\n" \
  "s_nop 0\n" \
  "s_nop 0\n" \
  "s_nop 0\n" \
  "s_nop 0\n" \
  "s_nop 0\n" \
  "s_nop 0\n" \
  "s_nop 0\n" \
  "s_nop 0\n" \
  "s_nop 0\n" \
  "s_nop 0\n" \
  "s_nop 0\n" \
  "s_nop 0\n" \
  "s_nop 0\n" \
  "s_nop 0\n" \
  "s_nop 0\n" \
  "s_nop 0\n" \
  "s_nop 0\n" \
  "s_nop 0\n" \
  "s_nop 0\n" \
  "s_nop 0\n" \
  "s_nop 0\n" \
  "s_nop 0\n" \
  "L_w2c_%=:\n" \
  "buffer_load_dwordx4 v[180:183], %9, %13, s53 offen offset:0 sc1\n" \
  "buffer_load_dwordx4 v[184:187], %9, %13, s53 offen offset:1024 sc1\n" \
  "buffer_load_dwordx4 v[188:191], %9, %13, s53 offen offset:2048 sc1\n" \
  "buffer_load_dwordx4 v[192:195], %9, %13, s53 offen offset:3072 sc1\n" \
  "buffer_load_dwordx4 v[196:199], %9, %13, s90 offen offset:0 sc1\n" \
  "buffer_load_dwordx4 v[200:203], %9, %13, s90 offen offset:1024 sc1\n" \
  "buffer_load_dwordx4 v[204:207], %9, %13, s90 offen offset:2048 sc1\n" \
  "buffer_load_dwordx4 v[208:211], %9, %13, s90 offen offset:3072 sc1\n" \
  "buffer_load_dwordx2 v[212:213], v15, %13, s56 offen offset:0 sc1\n" \
  "buffer_load_dwordx2 v[216:217], v15, %13, s56 offen offset:512 sc1\n" \
  "buffer_load_dwordx2 v[220:221], v15, %13, s56 offen offset:1024 sc1\n" \
  "buffer_load_dwordx2 v[224:225], v15, %13, s56 offen offset:1536 sc1\n" \
  "s_sub_u32 s42, s47, s43\n" \
  "s_cmp_lg_u32 s42, s94\n" \
  "s_cbranch_scc1 L_vpg_%=\n" \
  "L_vpd_%=:\n" \
  "s_barrier\n" \
  "ds_read_b128 v[156:159], %7 offset:10240\n" \
  "ds_read_b128 v[160:163], %7 offset:11264\n" \
  "ds_read_b128 v[164:167], %7 offset:12288\n" \
  "ds_read_b128 v[168:171], %7 offset:13312\n" \
  "ds_read_b128 v[172:175], %7 offset:14336\n" \
  "ds_read_b128 v[176:179], %7 offset:15360\n" \
  "v_permlane32_swap_b32 v72, v74\n" \
  "v_permlane32_swap_b32 v76, v78\n" \
  "s_nop 0\n" \
  "v_permlane16_swap_b32 v72, v74\n" \
  "v_permlane16_swap_b32 v76, v78\n" \
  "s_nop 0\n" \
  "v_permlane16_swap_b32 v72, v73\n" \
  "v_permlane16_swap_b32 v74, v75\n" \
  "v_permlane16_swap_b32 v76, v77\n" \
  "v_permlane16_swap_b32 v78, v79\n" \
  "v_mov_b64 v[52:53], 0\n" \
  "v_mov_b64 v[54:55], 0\n" \
  "v_mov_b64 v[56:57], 0\n" \
  "v_mov_b64 v[58:59], 0\n" \
  "v_mov_b64 v[60:61], 0\n" \
  "v_mov_b64 v[62:63], 0\n" \
  "v_mov_b64 v[64:65], 0\n" \
  "v_mov_b64 v[66:67], 0\n" \
  "s_mov_b32 s42, s50\n" \
  "s_mov_b32 s50, s51\n" \
  "s_mov_b32 s51, s52\n" \
  "s_mov_b32 s52, s42\n" \
  "s_mov_b32 s42, s53\n" \
  "s_mov_b32 s53, s54\n" \
  "s_mov_b32 s54, s55\n" \
  "s_mov_b32 s55, s42\n" \
  "s_mov_b32 s42, s56\n" \
  "s_mov_b32 s56, s57\n" \
  "s_mov_b32 s57, s58\n" \
  "s_mov_b32 s58, s42\n" \
  "s_mov_b32 s42, s61\n" \
  "s_mov_b32 s61, s62\n" \
  "s_mov_b32 s62, s63\n" \
  "s_mov_b32 s63, s42\n" \
  "s_mov_b32 s42, s90\n" \
  "s_mov_b32 s90, s91\n" \
  "s_mov_b32 s91, s92\n" \
  "s_mov_b32 s92, s42\n" \
  "s_waitcnt lgkmcnt(0)\n" \
  "s_waitcnt vmcnt(11)\n" \
  "v_smfmac_f32_16x16x64_bf16 v[80:83], v[180:183], v[84:91], %11\n" \
  "s_waitcnt vmcnt(10)\n" \
  "v_smfmac_f32_16x16x64_bf16 v[80:83], v[184:187], v[92:99], %11\n" \
  "s_waitcnt vmcnt(9)\n" \
  "v_smfmac_f32_16x16x64_bf16 v[80:83], v[188:191], v[100:107], %11\n" \
  "s_waitcnt vmcnt(8)\n" \
  "v_smfmac_f32_16x16x64_bf16 v[80:83], v[192:195], v[108:115], %11\n" \
  "s_waitcnt vmcnt(7)\n" \
  "v_smfmac_f32_16x16x64_bf16 v[80:83], v[196:199], v[116:123], %11\n" \
  "s_waitcnt vmcnt(6)\n" \
  "v_smfmac_f32_16x16x64_bf16 v[80:83], v[200:203], v[124:131], %11\n" \
  "s_waitcnt vmcnt(5)\n" \
  "v_smfmac_f32_16x16x64_bf16 v[80:83], v[204:207], v[132:139], %11\n" \
  "s_waitcnt vmcnt(4)\n" \
  "v_smfmac_f32_16x16x64_bf16 v[80:83], v[208:211], v[140:147], %11\n" \
  "s_waitcnt vmcnt(3)\n" \
  "v_smfmac_f32_16x16x64_bf16 v[80:83], v[212:215], v[72:79], %11\n" \
  "s_waitcnt vmcnt(2)\n" \
  "v_smfmac_f32_16x16x64_bf16 v[80:83], v[216:219], v[156:163], %11\n" \
  "s_barrier\n" \
  "s_waitcnt vmcnt(1)\n" \
  "v_smfmac_f32_16x16x64_bf16 v[80:83], v[220:223], v[164:171], %11\n" \
  "s_waitcnt vmcnt(0)\n" \
  "v_smfmac_f32_16x16x64_bf16 v[80:83], v[224:227], v[172:179], %11\n" \
  "L_pp_%=:\n" \
  "s_nop 7\n" \
  "v_pk_add_f32 v[80:81], v[80:81], v[82:83]\n" \
  "s_nop 0\n" \
  "v_cvt_pk_bf16_f32 v82, v80, v81\n" \
  "s_nop 1\n" \
  "v_mov_b32_dpp v83, v82 quad_perm:[1,0,3,2] row_mask:0xf bank_mask:0xf\n" \
  "v_perm_b32 v8, v83, v82, %12\n" \
  "v_cmpx_o_f32 vcc, v80, v81\n" \
  "buffer_store_dword v8, %10, %13, s61 offen sc1\n" \
  "s_mov_b64 exec, -1\n" \
  "s_cmp_eq_u64 vcc, -1\n" \
  "s_cbranch_scc0 L_ppr_%=\n" \
  "L_ppok_%=:\n" \
  "s_cmp_lt_u32 s43, 63\n" \
  "s_cbranch_scc1 L_step_%=\n" \
  "buffer_store_dword v8, %10, %13, %21 offen sc1\n" \
  "s_branch L_step_%=\n" \
  "L_ppr_%=:\n" \
  "v_max_u32 v10, v180, v181\n" \
  "v_max3_u32 v10, v10, v182, v183\n" \
  "v_max3_u32 v10, v10, v184, v185\n" \
  "v_max3_u32 v10, v10, v186, v187\n" \
  "v_max3_u32 v10, v10, v188, v189\n" \
  "v_max3_u32 v10, v10, v190, v191\n" \
  "v_max3_u32 v10, v10, v192, v193\n" \
  "v_max3_u32 v10, v10, v194, v195\n" \
  "v_max3_u32 v10, v10, v196, v197\n" \
  "v_max3_u32 v10, v10, v198, v199\n" \
  "v_max3_u32 v10, v10, v200, v201\n" \
  "v_max3_u32 v10, v10, v202, v203\n" \
  "v_max3_u32 v10, v10, v204, v205\n" \
  "v_max3_u32 v10, v10, v206, v207\n" \
  "v_max3_u32 v10, v10, v208, v209\n" \
  "v_max3_u32 v10, v10, v210, v211\n" \
  "v_max3_u32 v10, v10, v212, v213\n" \
  "v_max3_u32 v10, v10, v216, v217\n" \
  "v_max3_u32 v10, v10, v220, v221\n" \
  "v_max3_u32 v10, v10, v224, v225\n" \
  "v_min_u32 v8, -2, v8\n" \
  "v_cmp_eq_u32 vcc, -1, v10\n" \
  "s_cbranch_vccz L_pst_%=\n" \
  "s_sub_u32 s40, s40, 1\n" \
  "s_cbranch_scc1 L_ppto_%=\n" \
  "v_mov_b64 v[80:81], 0\n" \
  "v_mov_b64 v[82:83], 0\n" \
  "s_sub_u32 s39, s65, s36\n" \
  "s_mul_i32 s39, s39, s38\n" \
  "s_ashr_i32 s39, s39, 12\n" \
  "s_add_u32 s39, s39, s37\n" \
  "L_cwp_%=:\n" \
  "s_memtime s[88:89]\n" \
  "s_waitcnt lgkmcnt(0)\n" \
  "s_sub_u32 s42, s39, s88\n" \
  "s_cmp_gt_i32 s42, 900\n" \
  "s_cbranch_scc1 L_cwp_%=\n" \
  "buffer_store_dword v14, %10, %13, s61 offen sc1\n" \
  "s_memtime s[88:89]\n" \
  "s_memrealtime s[8:9]\n" \
  "s_nop 0\n" \
  "s_memrealtime s[10:11]\n" \
  "s_nop 0\n" \
  "s_memrealtime s[12:13]\n" \
  "s_nop 0\n" \
  "s_memrealtime s[14:15]\n" \
  "s_nop 0\n" \
  "s_memrealtime s[16:17]\n" \
  "s_nop 0\n" \
  "s_memrealtime s[18:19]\n" \
  "s_nop 0\n" \
  "s_memrealtime s[20:21]\n" \
  "s_nop 0\n" \
  "s_memrealtime s[22:23]\n" \
  "s_nop 0\n" \
  "s_memrealtime s[24:25]\n" \
  "s_nop 0\n" \
  "s_memrealtime s[26:27]\n" \
  "s_nop 0\n" \
  "s_memrealtime s[28:29]\n" \
  "s_nop 0\n" \
  "s_memrealtime s[30:31]\n" \
  "s_waitcnt lgkmcnt(0)\n" \
  "s_add_u32 s8, s8, s10\n" \
  "s_add_u32 s8, s8, s12\n" \
  "s_add_u32 s8, s8, s14\n" \
  "s_add_u32 s8, s8, s16\n" \
  "s_add_u32 s8, s8, s18\n" \
  "s_add_u32 s8, s8, s20\n" \
  "s_add_u32 s8, s8, s22\n" \
  "s_add_u32 s8, s8, s24\n" \
  "s_add_u32 s8, s8, s26\n" \
  "s_add_u32 s8, s8, s28\n" \
  "s_add_u32 s8, s8, s30\n" \
  "s_mov_b32 s36, s8\n" \
  "s_mov_b32 s37, s88\n" \
  "L_w0d_%=:\n" \
  "s_sub_u32 s39, s65, s36\n" \
  "s_add_u32 s65, s65, s41\n" \
  "s_add_u32 s47, s47, 1\n" \
  "s_cmp_gt_i32 s39, 0x20000\n" \
  "s_cbranch_scc1 L_w2d_%=\n" \
  "s_mul_i32 s39, s39, s38\n" \
  "s_ashr_i32 s39, s39, 12\n" \
  "s_add_u32 s39, s39, s37\n" \
  "L_w1d_%=:\n" \
  "s_memtime s[88:89]\n" \
  "s_waitcnt lgkmcnt(0)\n" \
  "s_sub_u32 s42, s39, s88\n" \
  "s_cmp_gt_i32 s42, 128\n" \
  "s_cbranch_scc1 L_w1d_%=\n" \
  "s_cmp_lt_i32 s42, -400\n" \
  "s_cbranch_scc1 L_w0d_%=\n" \
  "s_max_i32 s42, s42, 0\n" \
  "s_getpc_b64 s[88:89]\n" \
  "L_pcd_%=:\n" \
  "s_andn2_b32 s42, s42, 3\n" \
  "s_sub_u32 s42, L_w2d_%=-L_pcd_%=, s42\n" \
  "s_add_u32 s88, s88, s42\n" \
  "s_addc_u32 s89, s89, 0\n" \
  "s_setpc_b64 s[88:89]\n" \
  ".p2align 5\n" \
  "s_nop 0\n" \
  "s_nop 0\n" \
  "s_nop 0\n" \
  "s_nop 0\n" \
  "s_nop 0\n" \
  "s_nop 0\n" \
  "s_nop 0\n" \
  "s_nop 0\n" \
  "s_nop 0\n" \
  "s_nop 0\n" \
  "s_nop 0\n" \
  "s_nop 0\n" \
  "s_nop 0\n" \
  "s_nop 0\n" \
  "s_nop 0\n" \
  "s_nop 0\n" \
  "s_nop 0\n" \
  "s_nop 0\n" \
  "s_nop 0\n" \
  "s_nop 0\n" \
  "s_nop 0\n" \
  "s_nop 0\n" \
  "s_nop 0\n" \
  "s_nop 0\n" \
  "s_nop 0\n" \
  "s_nop 0\n" \
  "s_nop 0\n" \
  "s_nop 0\n" \
  "s_nop 0\n" \
  "s_nop 0\n" \
  "s_nop 0\n" \
  "s_nop 0\n" \
  "L_w2d_%=:\n" \
  "buffer_load_dwordx4 v[180:183], %9, %13, s55 offen offset:0 sc1\n" \
  "buffer_load_dwordx4 v[184:187], %9, %13, s55 offen offset:1024 sc1\n" \
  "buffer_load_dwordx4 v[188:191], %9, %13, s55 offen offset:2048 sc1\n" \
  "buffer_load_dwordx4 v[192:195], %9, %13, s55 offen offset:3072 sc1\n" \
  "buffer_load_dwordx4 v[196:199], %9, %13, s92 offen offset:0 sc1\n" \
  "buffer_load_dwordx4 v[200:203], %9, %13, s92 offen offset:1024 sc1\n" \
  "buffer_load_dwordx4 v[204:207], %9, %13, s92 offen offset:2048 sc1\n" \
  "buffer_load_dwordx4 v[208:211], %9, %13, s92 offen offset:3072 sc1\n" \
  "buffer_load_dwordx2 v[212:213], v15, %13, s58 offen offset:0 sc1\n" \
  "buffer_load_dwordx2 v[216:217], v15, %13, s58 offen offset:512 sc1\n" \
  "buffer_load_dwordx2 v[220:221], v15, %13, s58 offen offset:1024 sc1\n" \
  "buffer_load_dwordx2 v[224:225], v15, %13, s58 offen offset:1536 sc1\n" \
  "s_sub_u32 s94, s47, s43\n" \
  "s_waitcnt vmcnt(11)\n" \
  "v_smfmac_f32_16x16x64_bf16 v[80:83], v[180:183], v[84:91], %11\n" \
  "s_waitcnt vmcnt(10)\n" \
  "v_smfmac_f32_16x16x64_bf16 v[80:83], v[184:187], v[92:99], %11\n" \
  "s_waitcnt vmcnt(9)\n" \
  "v_smfmac_f32_16x16x64_bf16 v[80:83], v[188:191], v[100:107], %11\n" \
  "s_waitcnt vmcnt(8)\n" \
  "v_smfmac_f32_16x16x64_bf16 v[80:83], v[192:195], v[108:115], %11\n" \
  "s_waitcnt vmcnt(7)\n" \
  "v_smfmac_f32_16x16x64_bf16 v[80:83], v[196:199], v[116:123], %11\n" \
  "s_waitcnt vmcnt(6)\n" \
  "v_smfmac_f32_16x16x64_bf16 v[80:83], v[200:203], v[124:131], %11\n" \
  "s_waitcnt vmcnt(5)\n" \
  "v_smfmac_f32_16x16x64_bf16 v[80:83], v[204:207], v[132:139], %11\n" \
  "s_waitcnt vmcnt(4)\n" \
  "v_smfmac_f32_16x16x64_bf16 v[80:83], v[208:211], v[140:147], %11\n" \
  "s_waitcnt vmcnt(3)\n" \
  "v_smfmac_f32_16x16x64_bf16 v[80:83], v[212:215], v[72:79], %11\n" \
  "s_waitcnt vmcnt(2)\n" \
  "v_smfmac_f32_16x16x64_bf16 v[80:83], v[216:219], v[156:163], %11\n" \
  "s_waitcnt vmcnt(1)\n" \
  "v_smfmac_f32_16x16x64_bf16 v[80:83], v[220:223], v[164:171], %11\n" \
  "s_waitcnt vmcnt(0)\n" \
  "v_smfmac_f32_16x16x64_bf16 v[80:83], v[224:227], v[172:179], %11\n" \
  "s_branch L_pp_%=\n" \
  "L_ppto_%=:\n" \
  "s_mov_b32 s40, 0\n" \
  "L_pst_%=:\n" \
  "buffer_store_dword v8, %10, %13, s61 offen sc1\n" \
  "s_branch L_ppok_%=\n" \
  "L_vpg_%=:\n" \
  "s_mov_b32 s94, s42\n" \
  "buffer_store_dword v14, %10, %13, s62 offen sc1\n" \
  "s_branch L_vpd_%=\n" \
  "L_last_%=:\n" \
  "s_add_u32 s44, %22, 114688\n" \
  "buffer_store_dwordx4 v[68:71], %7, %13, s44 offen sc1\n" \
  "L_end_%=:\n" \
  "s_endpgm\n"
#define VALUE_CLOB \
  "v8", "v9", "v10", "v11", "v12", "v13", "v14", "v15", "v16", "v17", "v18", "v19", "v20", "v21", "v22", "v23", "v24", "v25", "v26", "v27", \
  "v28", "v29", "v30", "v31", "v32", "v33", "v34", "v35", "v36", "v37", "v38", "v39", "v40", "v41", "v42", "v43", "v44", "v45", "v46", "v47", \
  "v48", "v49", "v50", "v51", "v52", "v53", "v54", "v55", "v56", "v57", "v58", "v59", "v60", "v61", "v62", "v63", "v64", "v65", "v66", "v67", \
  "v68", "v69", "v70", "v71", "v72", "v73", "v74", "v75", "v76", "v77", "v78", "v79", "v80", "v81", "v82", "v83", "v84", "v85", "v86", "v87", \
  "v88", "v89", "v90", "v91", "v92", "v93", "v94", "v95", "v96", "v97", "v98", "v99", "v100", "v101", "v102", "v103", "v104", "v105", "v106", "v107", \
  "v108", "v109", "v110", "v111", "v112", "v113", "v114", "v115", "v116", "v117", "v118", "v119", "v120", "v121", "v122", "v123", "v124", "v125", "v126", "v127", \
  "v128", "v129", "v130", "v131", "v132", "v133", "v134", "v135", "v136", "v137", "v138", "v139", "v140", "v141", "v142", "v143", "v144", "v145", "v146", "v147", \
  "v148", "v149", "v150", "v151", "v152", "v153", "v154", "v155", "v156", "v157", "v158", "v159", "v160", "v161", "v162", "v163", "v164", "v165", "v166", "v167", \
  "v168", "v169", "v170", "v171", "v172", "v173", "v174", "v175", "v176", "v177", "v178", "v179", "v180", "v181", "v182", "v183", "v184", "v185", "v186", "v187", \
  "v188", "v189", "v190", "v191", "v192", "v193", "v194", "v195", "v196", "v197", "v198", "v199", "v200", "v201", "v202", "v203", "v204", "v205", "v206", "v207", \
  "v208", "v209", "v210", "v211", "v212", "v213", "v214", "v215", "v216", "v217", "v218", "v219", "v220", "v221", "v222", "v223", "v224", "v225", "v226", "v227", \
  "v228", "v229", "v230", "v231", "v232", "v233", "v234", "v235", "v236", "v237", "v238", "v239", "v240", "v241", "v242", "v243", "v244", "v245", "v246", "v247", \
  "v248", "v249", "v250", "v251", "v252", "v253", "v254", "v255", "a0", "a1", "a2", "a3", "a4", "a5", "a6", "a7", "a8", "a9", "a10", "a11", \
  "a12", "a13", "a14", "a15", "a16", "a17", "a18", "a19", "a20", "a21", "a22", "a23", "a24", "a25", "a26", "a27", "a28", "a29", "a30", "a31", \
  "a32", "a33", "a34", "a35", "a36", "a37", "a38", "a39", "a40", "a41", "a42", "a43", "a44", "a45", "a46", "a47", "a48", "a49", "a50", "a51", \
  "a52", "a53", "a54", "a55", "a56", "a57", "a58", "a59", "a60", "a61", "a62", "a63", "a64", "a65", "a66", "a67", "a68", "a69", "a70", "a71", \
  "a72", "a73", "a74", "a75", "a76", "a77", "a78", "a79", "a80", "a81", "a82", "a83", "a84", "a85", "a86", "a87", "a88", "a89", "a90", "a91", \
  "a92", "a93", "a94", "a95", "a96", "a97", "a98", "a99", "a100", "a101", "a102", "a103", "a104", "a105", "a106", "a107", "a108", "a109", "a110", "a111", \
  "a112", "a113", "a114", "a115", "a116", "a117", "a118", "a119", "a120", "a121", "a122", "a123", "a124", "a125", "a126", "a127", "a128", "a129", "a130", "a131", \
  "a132", "a133", "a134", "a135", "a136", "a137", "a138", "a139", "a140", "a141", "a142", "a143", "a144", "a145", "a146", "a147", "a148", "a149", "a150", "a151", \
  "a152", "a153", "a154", "a155", "a156", "a157", "a158", "a159", "a160", "a161", "a162", "a163", "a164", "a165", "a166", "a167", "a168", "a169", "a170", "a171", \
  "a172", "a173", "a174", "a175", "a176", "a177", "a178", "a179", "a180", "a181", "a182", "a183", "a184", "a185", "a186", "a187", "a188", "a189", "a190", "a191", \
  "a192", "a193", "a194", "a195", "a196", "a197", "a198", "a199", "a200", "a201", "a202", "a203", "a204", "a205", "a206", "a207", "a208", "a209", "a210", "a211", \
  "a212", "a213", "a214", "a215", "a216", "a217", "a218", "a219", "a220", "a221", "a222", "a223", "a224", "a225", "a226", "a227", "a228", "a229", "a230", "a231", \
  "a232", "a233", "a234", "a235", "a236", "a237", "a238", "a239", "a240", "a241", "a242", "a243", "a244", "a245", "a246", "a247", "a248", "a249", "a250", "a251", \
  "a252", "a253", "a254", "a255", "s8", "s9", "s10", "s11", "s12", "s13", "s14", "s15", "s16", "s17", "s18", "s19", "s20", "s21", "s22", "s23", \
  "s24", "s25", "s26", "s27", "s28", "s29", "s30", "s31", "s36", "s37", "s38", "s39", "s40", "s41", "s42", "s43", "s44", "s45", "s46", "s47", \
  "s48", "s49", "s50", "s51", "s52", "s53", "s54", "s55", "s56", "s57", "s58", "s61", "s62", "s63", "s64", "s65", "s88", "s89", "s90", "s91", \
  "s92", "s93", "s94", "s95", "vcc", "scc", "memory"

__global__ void __launch_bounds__(512, 2)
prep_kernel(const u16* __restrict__ x, const u16* __restrict__ mem0, const u16* __restrict__ Wq,
            const u16* __restrict__ Wk, const u16* __restrict__ Wv, const u16* __restrict__ Wo,
            const u16* __restrict__ w_r, const u16* __restrict__ gate_w, char* ws) {
  extern __shared__ __attribute__((aligned(16))) char smem[];
  const int b = blockIdx.x, tid = threadIdx.x, lane = tid & 63;
  const int wave = __builtin_amdgcn_readfirstlane(tid >> 6);
  const int mat = (b & 7) * 4 + (b >> 6), sub = (b >> 3) & 7;
  const int isB = mat & 1, h = mat >> 1, dh = sub >> 2, nq = sub & 3;
  const int c = lane & 15, g = lane >> 4;
  const unsigned ring = ring_off(ws);
  const rsrc_t rs = __builtin_amdgcn_make_buffer_rsrc(ws, 0, WS_USED, 0x00020000);
  const bool prober = wave == 7 && ((b & 7) == 0 || (b & 7) == XV);
  const int pside = (b & 7) != 0;
  const int ppair = ((b >> 3) + (b >> 5)) & 3;
  const unsigned probe0 = ring + 7 * 32768 + 2048 + pside * 1024 + (b >> 5) * 128 + ppair * 8192;
  {
    constexpr unsigned NCH = (OFF_SENT_END - OFF_SENT) / 16;
    static_assert(NCH <= 256 * 512, "one store per thread");
    const unsigned i = (wave * 256 + b) * 64 + lane, off = OFF_SENT + i * 16, ro = off - ring;
    if (i < NCH && ((ro >> 15) != 7 || (ro & 0x800) == 0))
      __builtin_amdgcn_raw_buffer_store_b128((u32x4){SENT, SENT, SENT, SENT}, rs, off, 0, K1_AUX);
  }
  const u16* W1 = (isB ? Wv : Wq) + h * 256;
  const int db = 7 * dh + wave;
  u32x4 xkv, ex[8];
  gld16(xkv, x + ((b >> 2) * 32 + 16 * (wave >> 2) + c) * DM + 112 * (b & 3) + 32 * (wave & 3) + 8 * ((wave & 3) == 3 ? (g & 1) : g));
  if (wave >= 6) {
    const u16* p = wave == 6 ? W1 + (32 * db + 2 + 16 * (c >> 3) + 4 * ((c >> 2) & 1) + 8 * ((c >> 1) & 1) + (c & 1)) * 4096 + g * 8
                             : x + (b * 8 + (lane >> 3)) * DM + (lane & 7) * 8;
    const int stp = wave == 6 ? 32 : 64;
#pragma unroll
    for (int i = 0; i < 7; ++i) gld16(ex[i], p + i * stp);
    gld16(ex[7], prober ? (const u16*)(ws + probe0 + (lane & 1) * 4096) : p + 7 * 32);
  }

  {
    const int q = wave & 3, par = wave >> 2, rc = 4 * q + g, sw = (c ^ rc) * 8;
    const u16* p1 = W1 + (224 * dh + 16 * (rc >> 3) + 4 * ((rc >> 2) & 1) + 8 * ((rc >> 1) & 1) + (rc & 1)) * 4096 + sw;
    const u16* src[10];
#pragma unroll
    for (int i = 0; i < 4; ++i) {
      const int t = par + 2 * i;
      if (!isB) {
        src[i] = Wk + h * 256 + sw + (112 * nq + (i < 3 ? 32 * i + 2 * par + 8 * (rc >> 2) + 4 * ((rc >> 1) & 1) + (rc & 1) : 96 + rc)) * 4096;
      } else {
        const int f = (4 * t + q) * 64 + lane, rho = f / 14, piece = f - rho * 14;
        const int e = (rho & ~31) + 8 * ((rho & 15) >> 2) + 4 * ((rho & 31) >> 4) + (rho & 3);
        src[i] = Wo + (h * 256 + e) * DM + nq * 112 + piece * 8;
      }
    }
    if (par) src[3] = p1;
#pragma unroll
    for (int i = 4; i < 10; ++i) {
      const int tw = par + 2 * i - 7;
      src[i] = p1 + (32 * (tw >> 1) + 2 * (tw & 1)) * 4096;
    }
    const int hs = isB ? 128 * DM : 128;
#pragma unroll
    for (int hf = 0; hf < 2; ++hf)
#pragma unroll
      for (int i = 0; i < 10; ++i)
        glds16(src[i] + hf * ((i < 3 || (i == 3 && !par)) ? hs : 128), hf * 81920 + (par + 2 * i) * 4096 + q * 1024);
  }
  asm volatile("s_waitcnt vmcnt(10)\n\ts_barrier" : "+v"(xkv), "+v"(ex[0]), "+v"(ex[1]), "+v"(ex[2]), "+v"(ex[3]), "+v"(ex[4]), "+v"(ex[5]), "+v"(ex[6]), "+v"(ex[7]) :: "memory");
  __builtin_amdgcn_raw_buffer_store_b128(xkv, rs, tid * 16 + OFF_XK + b * 8192, 0, K1_AUX);
  if (wave == 7) {
    u32x4 wrv[7], gwv[7];
#pragma unroll
    for (int i = 0; i < 7; ++i) {
      wrv[i] = *(const u32x4*)(w_r + (i * 8 + (lane & 7)) * 8);
      gwv[i] = *(const u32x4*)(gate_w + (i * 8 + (lane & 7)) * 8);
    }
    unsigned best[2] = {0xffffffffu, 0xffffffffu};
#pragma unroll 1
    for (int r = 0; r < 2; ++r) {
      if (prober) {
        if (lane < 2) st4(rs, lane * 4096, probe0, 0u);
#pragma unroll
        for (int k = 0; k < 2; ++k) {
          unsigned long long t0, t1;
          asm volatile("s_waitcnt vmcnt(0)\n\ts_memtime %0\n\ts_waitcnt lgkmcnt(0)" : "=s"(t0) :: "memory");
          unsigned v = ld4(rs, 0, probe0 + k * 4096);
          asm volatile("s_waitcnt vmcnt(0)\n\ts_memtime %0\n\ts_waitcnt lgkmcnt(0)" : "=s"(t1), "+v"(v) :: "memory");
          best[k] = min(best[k], (unsigned)(t1 - t0));
        }
      }
      if (r == 0) asm volatile("s_waitcnt vmcnt(0)\n\ts_barrier" ::: "memory");
    }
    if (prober && lane < 2) *(unsigned*)(ws + OFF_LAT + (pside * 64 + (b >> 5) * 8 + ppair * 2 + lane) * 4) = lane ? best[1] : best[0];
    float sr = 0.f, sg = 0.f;
#pragma unroll
    for (int i = 0; i < 7; ++i) {
      const u32x4 wr = wrv[i], gw = gwv[i];
#pragma unroll
      for (int q = 0; q < 4; ++q) {
        const float x0 = bflo(ex[i][q]), x1 = bfhi(ex[i][q]);
        sr = fmaf(x0, bflo(wr[q]), sr);
        sr = fmaf(x1, bfhi(wr[q]), sr);
        const unsigned pr = pk(x0 * bflo(gw[q]), x1 * bfhi(gw[q]));
        sg += bflo(pr) + bfhi(pr);
      }
    }
    sr += dpp<0xB1>(sr); sr += dpp<0x4E>(sr); sr += dpp<0x141>(sr);
    sg += dpp<0xB1>(sg); sg += dpp<0x4E>(sg); sg += dpp<0x141>(sg);
    if ((lane & 7) == 0) {
      *(float*)(ws + OFF_R + (b * 8 + (lane >> 3)) * 4) = rbf(sr);
      *(float*)(ws + OFF_G + (b * 8 + (lane >> 3)) * 4) = rbf(sigmoidf_(rbf(sg)));
    }
    if (b >= 1 && b < 8) {
      u32x4 v = *(const u32x4*)(mem0 + (2 * (c >> 2) + (c & 1)) * DM + ((b - 1) * 8 + 2 * g + ((c >> 1) & 1)) * 8);
#pragma unroll
      for (int q = 0; q < 4; ++q) v[q] = canon(v[q]);
      *(u32x4*)(ws + OFF_MEM0 + (b - 1) * 1024 + lane * 16) = v;
    }
    return;
  }

  f32x4 acc[7][2];
#pragma unroll
  for (int t = 0; t < 7; ++t) { acc[t][0] = (f32x4){0, 0, 0, 0}; acc[t][1] = (f32x4){0, 0, 0, 0}; }
  const float osc = isB ? 1.f : SM_C;
#pragma unroll
  for (int hf = 0; hf < 2; ++hf) {
    if (hf) asm volatile("s_waitcnt vmcnt(0)\n\ts_barrier" ::: "memory");
    const char* hb = smem + hf * 81920;
    u32x4 a[2][4];
#pragma unroll
    for (int st = 0; st < 4; ++st) {
      const char* ar = hb + 28672 + wave * 8192 + c * 256 + (((4 * st + g) ^ c) * 16);
      a[0][st] = *(const u32x4*)ar;
      a[1][st] = *(const u32x4*)(ar + (wave == 6 ? 0 : 4096));
      if (wave == 6) a[1][st] = ex[4 * hf + st];
    }
    const char* base = hb + (4 * g + (c >> 2)) * 224 + (c & 3) * 8;
#define K1_TILES(BOP) \
    _Pragma("unroll") for (int t = 0; t < 7; ++t) { \
      _Pragma("unroll") for (int st = 0; st < 4; ++st) { \
        const bf16x8 bo = BOP; \
        acc[t][0] = mf32(asb8(a[0][st]), bo, acc[t][0]); \
        acc[t][1] = mf32(asb8(a[1][st]), bo, acc[t][1]); \
      } \
      if (hf) { \
        const int ct = 7 * nq + t; \
        const unsigned dst = isB ? OFF_WB + (((ct >> 1) * 4 + (h >> 2)) * 8 + (h & 3) * 2 + (ct & 1)) * TILE_B \
                                 : OFF_WA + ((h * 4 + nq) * 7 + (t == 5 ? 6 : (t == 6 ? 5 : t))) * TILE_B; \
        const f32x4 u = acc[t][0] * osc, v = acc[t][1] * osc; \
        __builtin_amdgcn_raw_buffer_store_b128((u32x4){pk(u[0], u[1]), pk(u[2], u[3]), pk(v[0], v[1]), pk(v[2], v[3])}, rs, \
                                               lane * 16 + dst + db * 1024, 0, K1_AUX); \
      } \
    }
    if (!isB) {
      K1_TILES(*(const bf16x8*)(hb + t * 4096 + c * 256 + (((4 * st + g) ^ c) * 16)))
    } else {
      K1_TILES(trpair(base + st * 32 * 224 + t * 32))
    }
  }
}

constexpr int XP_SG = 528, XP_ST = 1056, XP_SK = 2208;
__device__ __forceinline__ void fetch_x(const u16* __restrict__ x, int s, int tid, u32x4 (&va)[4], u32x4 (&vb)[4]) {
#pragma unroll
  for (int i = 0; i < 4; ++i) {
    const int u = min(tid + 256 * i, 895);
    va[i] = *(const u32x4*)(x + s * 32 * DM + u * 16);
    vb[i] = *(const u32x4*)(x + s * 32 * DM + u * 16 + 8);
  }
}
template <bool KEYMAP>
__device__ __forceinline__ void put_x(char* xp, int tid, const u32x4 (&va)[4], const u32x4 (&vb)[4]) {
#pragma unroll
  for (int i = 0; i < 4; ++i) {
    const int u = min(tid + 256 * i, 895);
    const int row = u / 28, blk = u - row * 28;
    const int tt = KEYMAP ? ((row >> 2) & 1) : (row >> 4);
    const int lrow = KEYMAP ? (4 * (row >> 3) + (row & 3)) : (row & 15);
    char* p = xp + (blk >> 1) * XP_SK + tt * XP_ST + (blk & 1) * XP_SG + lrow * 16;
    *(u32x4*)p = (u32x4){va[i][0], vb[i][0], va[i][1], vb[i][1]};
    *(u32x4*)(p + 256) = (u32x4){va[i][2], vb[i][2], va[i][3], vb[i][3]};
  }
}

template <bool KEYMAP>
__device__ __forceinline__ void stage_x(const u16* __restrict__ x, int s, char* xp, int tid) {
  u32x4 va[4], vb[4];
  fetch_x(x, s, tid, va, vb);
  put_x<KEYMAP>(xp, tid, va, vb);
}

__device__ __forceinline__ int argmax_r(const char* ws, char* scratch, int tid, float& rbest) {
  const float* R = (const float*)(ws + OFF_R);
  const f32x4 v0 = *(const f32x4*)(R + tid * 8), v1 = *(const f32x4*)(R + tid * 8 + 4);
  float bv = -INFINITY; int bi = tid * 8;
#pragma unroll
  for (int q = 0; q < 8; ++q) {
    const float v = q < 4 ? v0[q] : v1[q - 4];
    if (v > bv) { bv = v; bi = tid * 8 + q; }
  }
#pragma unroll
  for (int o = 32; o >= 1; o >>= 1) {
    const float ov = __shfl_xor(bv, o); const int oi = __shfl_xor(bi, o);
    if (ov > bv || (ov == bv && oi < bi)) { bv = ov; bi = oi; }
  }
  if ((tid & 63) == 0) { ((float*)scratch)[(tid >> 6) * 2] = bv; ((int*)scratch)[(tid >> 6) * 2 + 1] = bi; }
  __syncthreads();
  bv = ((float*)scratch)[0]; bi = ((int*)scratch)[1];
#pragma unroll
  for (int w = 1; w < 4; ++w) {
    const float ov = ((float*)scratch)[w * 2]; const int oi = ((int*)scratch)[w * 2 + 1];
    if (ov > bv) { bv = ov; bi = oi; }
  }
  __syncthreads();
  rbest = R[bi];
  return bi;
}

__device__ __forceinline__ unsigned latch_cycles(unsigned rstar) {
  for (int i = 0; i < 256; ++i)
    if ((int)(rstar - (unsigned)__builtin_amdgcn_s_memrealtime()) < 8) break;
  unsigned long long r[14], m0;
  asm volatile(
      "s_memtime %14\n\t"
      "s_memrealtime %0\n\ts_nop 0\n\t"
      "s_memrealtime %1\n\ts_nop 0\n\t"
      "s_memrealtime %2\n\ts_nop 0\n\t"
      "s_memrealtime %3\n\ts_nop 0\n\t"
      "s_memrealtime %4\n\ts_nop 0\n\t"
      "s_memrealtime %5\n\ts_nop 0\n\t"
      "s_memrealtime %6\n\ts_nop 0\n\t"
      "s_memrealtime %7\n\ts_nop 0\n\t"
      "s_memrealtime %8\n\ts_nop 0\n\t"
      "s_memrealtime %9\n\ts_nop 0\n\t"
      "s_memrealtime %10\n\ts_nop 0\n\t"
      "s_memrealtime %11\n\ts_nop 0\n\t"
      "s_memrealtime %12\n\ts_nop 0\n\t"
      "s_memrealtime %13\n\ts_nop 0\n\t"
      "s_waitcnt lgkmcnt(0)"
      : "=&s"(r[0]), "=&s"(r[1]), "=&s"(r[2]), "=&s"(r[3]), "=&s"(r[4]), "=&s"(r[5]), "=&s"(r[6]), "=&s"(r[7]), "=&s"(r[8]), "=&s"(r[9]), "=&s"(r[10]), "=&s"(r[11]), "=&s"(r[12]), "=&s"(r[13]), "=&s"(m0)
      :: "memory");
  unsigned n = 0, re = (unsigned)r[13];
#pragma unroll
  for (int i = 13; i >= 1; --i) {
    const bool same = (unsigned)r[i] == (unsigned)r[0];
    n += same ? 1u : 0u;
    re = same ? re : (unsigned)r[i];
  }
  const unsigned org = (unsigned)m0 + 8 * n - (unsigned)(((int)(re - rstar) * 95) >> 2);
  return org;
}

__device__ __forceinline__ unsigned to_units(unsigned cyc, unsigned fab, unsigned rho) {
  return (cyc - fab) * 4096 / rho + fab * 4096 / 8150;
}

__device__ __forceinline__ void wait_final(rsrc_t rs, int lag, int& budget) {
  unsigned t;
  for (;;) {
    t = __builtin_amdgcn_readfirstlane(ld4(rs, 0, OFF_FIN));
    if (t != SENT) break;
    if (--budget < 0) return;
    __builtin_amdgcn_s_sleep(4);
    asm volatile("" ::: "memory");
  }
  for (int i = 0; i < 256 && (int)(t + lag - (unsigned)__builtin_amdgcn_s_memrealtime()) > 0; ++i) {}
}

__device__ __forceinline__ int fresh_lane() {
  int l;
  asm volatile("v_mbcnt_lo_u32_b32 %0, -1, 0\n\tv_mbcnt_hi_u32_b32 %0, -1, %0" : "=v"(l));
  return l & 63;
}

__global__ void __launch_bounds__(256, 1)
main_kernel(const u16* __restrict__ x, const u16* __restrict__ w_gate, const u16* __restrict__ w_up,
            const u16* __restrict__ w_down, u16* __restrict__ out, char* ws
            ) {
  extern __shared__ __attribute__((aligned(16))) char smem[];
  const int wave = __builtin_amdgcn_readfirstlane(threadIdx.x >> 6);
  const int xg = blockIdx.x & 7, bi = blockIdx.x >> 3;
  const rsrc_t rs = __builtin_amdgcn_make_buffer_rsrc(ws, 0, WS_USED, 0x00020000);
  int budget = POLL_BUDGET;

  int role = -1, team = -1, jj = bi;
  if (xg == 0) {
    if (bi < 16) role = bi;
  } else if (xg == XV) {
    if (bi < 28) role = 16 + (bi >> 1);
  } else if (xg >= 4) {
    team = 99;
    if (bi < 7 * NTB) { jj = (xg - 4) * 7 + bi % 7; role = 16 + (jj >> 1); team = bi / 7; }
    else if (bi >= 16) { role = (xg - 4) + 4 * ((bi - 16) & 3); team = (bi - 16) >> 2; }
  } else {
    team = bi < 28 ? bi * 2 + (xg >> 1) : 99;
  }

  if (role < 0) {
    if (xg == 0 && bi == 30) {
      if (wave != 0) return;
      const int lane = fresh_lane();
      const unsigned r0 = ((unsigned)__builtin_amdgcn_s_memrealtime() + 32) & ~3u;
      unsigned ls = min(*(const unsigned*)(ws + OFF_LAT + lane * 4), 0xffffu);
      unsigned lv = min(*(const unsigned*)(ws + OFF_LAT + 256 + lane * 4), 0xffffu);
#pragma unroll
      for (int o = 8; o <= 32; o <<= 1) { ls = min(ls, (unsigned)__shfl_xor((int)ls, o)); lv = min(lv, (unsigned)__shfl_xor((int)lv, o)); }
      unsigned v = ls + lv;
      v += (unsigned)__shfl_xor((int)v, 1);
      v = (v << 3) | (lane & 6);
      v = min(v, (unsigned)__shfl_xor((int)v, 2));
      v = min(v, (unsigned)__shfl_xor((int)v, 4));
      const int dd = (int)(ls - lv), pr = v & 6;
      unsigned b0 = pr + (__shfl(dd, pr) <= __shfl(dd, pr + 1) ? 0 : 1), b1 = b0 ^ 1;
      const unsigned pi = GRID_PI, phi = GRID_PHI, t0 = GRID_T0;
      const int sl = lane / 5, it = lane - sl * 5;
      const unsigned cp = (P_NEAR_S ? b0 : b1) * 4096, cm = (P_NEAR_S ? b1 : b0) * 4096;
      const unsigned wv = ring_off(ws) + (it < 2 ? (2 * sl + it) * 32768 + cp : (it == 2 ? (6 + (sl >> 1)) * 32768 + (sl & 1) * 2048 + cp : (2 * sl + it - 3) * 32768 + cm));
      if (lane < 15) st4(rs, lane * 4, OFF_CFG, wv);
      unsigned acc = 0;
#pragma unroll
      for (int i = 0; i < 8; ++i) acc |= __builtin_amdgcn_raw_buffer_load_b32(rs, lane * 128, OFF_XK + (4 + i) * 8192, 0);
      unsigned long long ra = __builtin_amdgcn_s_memrealtime(), ma = __builtin_amdgcn_s_memtime(), rb = ra, mb = ma;
      for (;;) {
        const unsigned f0 = ld4(rs, min(lane, 55) * 64, OFF_VF), f1 = ld4(rs, (lane & 15) * 64, OFF_RDY);
        const unsigned rn = (unsigned)__builtin_amdgcn_s_memrealtime();
        if (rn - (unsigned)rb >= 96) { ra = rb; ma = mb; rb = rn + 8; mb = latch_cycles(rn + 8); }
        if (__builtin_amdgcn_ballot_w64(f0 != 1u || f1 != 1u) == 0) break;
        if (--budget < 0) break;
        asm volatile("" ::: "memory");
      }
      unsigned tn = ((unsigned)__builtin_amdgcn_s_memrealtime() - r0) * 12 + t0 - 2000;
      const unsigned rho = min(max((unsigned)(mb - ma) * 1024u / (3u * max((unsigned)(rb - ra), 1u)), 4096u), 12288u);
      unsigned piu = to_units(pi, FAB_PI, rho * 4096 / DERATE), phu = to_units(phi, FAB_PHI, rho * 4096 / DERATE);
      if (lane < 4) st4(rs, lane * 4, OFF_CFG + 384, lane == 0 ? canon(r0 * 12 + tn) : (lane == 1 ? piu : (lane == 2 ? phu : rho)));
      unsigned pic = pi, phc = phi, rmin = rho, mprev = 0, rprev = 0, lost = SENT, pnew = piu, msg = 0;
      int nsw = -1, cool = 1, ent = 0, nxt = 3, nloss = -100;
      bool fin = false;
      for (int n = 0; n < 1000; ++n) {
        const unsigned r = r0 + (((unsigned)(tn + TOUCH_AT) / 12) & ~3u);
        if (n == nsw) piu = pnew;
        tn += piu;
        const unsigned mm = latch_cycles(r);
        if (msg != 0 && lane == 0) st4(rs, (ent - 1) * 4, OFF_CFG + 128, msg);
        const unsigned lw = ld4(rs, 0, OFF_CFG + 256);
        for (const int lim = min(n - (lost == SENT ? 0 : (int)lost) + TOUCH_AHEAD, NSEG - 2); nxt <= lim; ++nxt) {
#pragma unroll
          for (int i = 0; i < 4; ++i) acc |= __builtin_amdgcn_raw_buffer_load_b32(rs, lane * 128, OFF_XK + (nxt * 4 + i) * 8192, 0);
        }
        if (msg != 0) { __builtin_amdgcn_s_sleep(2); acc |= ld4(rs, 0, OFF_CFG + 128); }
        msg = 0;
        const unsigned lr = __builtin_amdgcn_readfirstlane(lw);
        if (lr == 0x7fffffffu) break;
        const unsigned l = lr == SENT ? SENT : (lr & 0xffffu);
        if (n > 0) rmin = min(rmin + CTL_RDEC, min(max((mm - mprev) * 1024u / (3u * (r - rprev)), 4096u), 12288u));
        mprev = mm; rprev = r;
        const int left = NSEG - 2 - (n - (l == SENT ? 0 : (int)l));
        if (!fin && left <= 1) {
          fin = true;
          if (lane == 0) st4(rs, 0, OFF_FIN, canon(r0 + (tn + max(left, 0) * piu) / 12));
        }
        const bool loss = l != lost;
        lost = l;
        if (n >= cool && ent < CTL_MAX) {
          if (loss && n > CTL_FIRST + 1 && n - nloss <= CTL_WIN) { pic += CTL_DPI; phc += (lr >> 16) ? 0 : CTL_DPI; }
          const int dp = max(-400, min(400, (int)(to_units(pic, FAB_PI, rmin) - piu))), df = max(-400, min(400, (int)(to_units(phc, FAB_PHI, rmin) - phu)));
          if (dp >= CTL_DEAD || (dp <= -CTL_DEAD && n >= CTL_FIRST)) {
            msg = (unsigned)(n + 4) | ((unsigned)(dp + 512) << 10) | ((unsigned)(df + 512) << 21);
            nsw = n + 4; pnew = piu + dp; phu += df; cool = n + 6; ++ent;
          }
        }
        if (loss) nloss = n;
        asm volatile("" :: "v"(acc));
      }
      if (!fin && lane == 0) st4(rs, 0, OFF_FIN, canon(r0));
      return;
    }
    if (team < 0 || team >= 56) return;
    float rbest;
    const int istar = __builtin_amdgcn_readfirstlane(argmax_r(ws, smem, wave * 64 + fresh_lane(), rbest));
    if (team < 28) {
    const int tid = wave * 64 + fresh_lane();
    float* xs = (float*)(smem + 64);
    float* red = (float*)(smem + 2048);
    float* hb = (float*)(smem + 2048 + 32 * 128 * 4);
    for (int i = tid; i < DM; i += 256) xs[i] = bf2f(x[istar * DM + i]);
    __syncthreads();
    const int f0 = team * 64, fc = tid & 7, dg = tid >> 3;
    float ag[8], au[8];
#pragma unroll
    for (int q = 0; q < 8; ++q) { ag[q] = 0.f; au[q] = 0.f; }
#pragma unroll
    for (int i = 0; i < 14; ++i) {
      const int d = dg * 14 + i;
      const u32x4 vg = *(const u32x4*)(w_gate + d * FF + f0 + fc * 8), vu = *(const u32x4*)(w_up + d * FF + f0 + fc * 8);
      const float xv = xs[d];
#pragma unroll
      for (int q = 0; q < 4; ++q) {
        ag[2 * q] = fmaf(xv, bflo(vg[q]), ag[2 * q]); ag[2 * q + 1] = fmaf(xv, bfhi(vg[q]), ag[2 * q + 1]);
        au[2 * q] = fmaf(xv, bflo(vu[q]), au[2 * q]); au[2 * q + 1] = fmaf(xv, bfhi(vu[q]), au[2 * q + 1]);
      }
    }
#pragma unroll
    for (int q = 0; q < 8; ++q) { red[dg * 128 + fc * 8 + q] = ag[q]; red[dg * 128 + 64 + fc * 8 + q] = au[q]; }
    __syncthreads();
    if (tid < 64) {
      float sg = 0.f, su = 0.f;
      for (int i = 0; i < 32; ++i) { sg += red[i * 128 + tid]; su += red[i * 128 + 64 + tid]; }
      sg = rbf(sg); su = rbf(su);
      hb[tid] = rbf(rbf(sg * sigmoidf_(sg)) * su);
    }
    __syncthreads();
    if (tid < 224) {
      const int cc = tid % 56, fg = tid / 56;
      float ay[8];
#pragma unroll
      for (int q = 0; q < 8; ++q) ay[q] = 0.f;
#pragma unroll
      for (int i = 0; i < 16; ++i) {
        const u32x4 vd = *(const u32x4*)(w_down + (f0 + fg * 16 + i) * DM + cc * 8);
        const float hv = hb[fg * 16 + i];
#pragma unroll
        for (int q = 0; q < 4; ++q) { ay[2 * q] = fmaf(hv, bflo(vd[q]), ay[2 * q]); ay[2 * q + 1] = fmaf(hv, bfhi(vd[q]), ay[2 * q + 1]); }
      }
#pragma unroll
      for (int q = 0; q < 8; ++q) red[fg * DM + cc * 8 + q] = ay[q];
    }
    __syncthreads();
    for (int i = tid; i < DM; i += 256) {
      const float v = (red[i] + red[DM + i]) + (red[2 * DM + i] + red[3 * DM + i]);
      st4(rs, OFF_YP + (team * DM + i) * 4, 0, canon(__float_as_uint(v)));
    }
    }
    __syncthreads();
    const int j = team % 14;
    char* RED = smem;
    f32x4 upd = {0, 0, 0, 0};
    if ((istar >> 5) % NTO == team / 14) {
      const int q8 = fresh_lane() & 7;
      u32x4 yv[28];
      for (;;) {
        unsigned mx = 0;
#pragma unroll
        for (int i = 0; i < 28; ++i) yv[i] = ld16(rs, q8 * 16, OFF_YP + (i * DM + j * 32) * 4);
#pragma unroll
        for (int i = 0; i < 28; ++i) mx = umax4(mx, yv[i]);
        if (__builtin_amdgcn_ballot_w64(mx == SENT) == 0) break;
        if (--budget < 0) break;
        __builtin_amdgcn_s_sleep(8);
        asm volatile("" ::: "memory");
      }
#pragma unroll
      for (int i = 0; i < 28; ++i) upd += __builtin_bit_cast(f32x4, yv[i]);
      const float rsel = rbf(sigmoidf_(rbest));
#pragma unroll
      for (int i = 0; i < 4; ++i) upd[i] = rbf(rbf(upd[i]) * rsel);
    }
    for (int s = team / 14; s < NSEG; s += NTO) {
      const int lane = fresh_lane(), tid = wave * 64 + lane, c = lane & 15, g = lane >> 4;
      const int tok = tid >> 3, q8 = tid & 7;
      const float gt = *(const float*)(ws + OFF_G + (s * 32 + tok) * 4);
      const u32x2 xb = *(const u32x2*)(x + (s * 32 + tok) * DM + j * 32 + q8 * 4);
      for (;;) {
        if (__builtin_amdgcn_ballot_w64(ld4(rs, (lane & 1) * 64, OFF_VF + (s * 28 + 2 * j) * 64) != 1u) == 0) break;
        if (--budget < 0) break;
        __builtin_amdgcn_s_sleep(SL_TOK);
        asm volatile("" ::: "memory");
      }
      asm volatile("" ::: "memory");
      u32x4 vo[8];
#pragma unroll
      for (int t = 0; t < 8; ++t) vo[t] = ld16(rs, lane * 16 + t * 1024, OFF_VB + ((s * 14 + j) * 4 + wave) * 8192);
      float bv[4] = {bflo(xb[0]), bfhi(xb[0]), bflo(xb[1]), bfhi(xb[1])};
      if (s * 32 + tok == istar) {
#pragma unroll
        for (int i = 0; i < 4; ++i) bv[i] = rbf(bv[i] + upd[i]);
      }
      {
        const bool pf = (lane & 4) == 0;
        const unsigned fo = pf ? OFF_PTF + (s * 16 + 4 * wave + (lane & 3)) * 64 : OFF_WF + (s * 28 + 2 * j + (lane & 1)) * 64;
                while (s < PT_ARM) {
          const unsigned f = ld4(rs, fo, 0);
          if (__builtin_amdgcn_ballot_w64(f != 1u) == 0) break;
          if (--budget < 0) break;
          __builtin_amdgcn_s_sleep(SL_TOK);
          asm volatile("" ::: "memory");
        }
      }
      while (PREPOLL && s >= PT_ARM) {
        const unsigned wo0 = OFF_WTA + (s - PT_ARM) * 28 * 4096 + ((2 * j + (lane & 1)) * 4 + wave) * 1024;
        const unsigned f = ld4(rs, (lane & 4) ? wo0 : pt_off(s) + (4 * wave + (lane & 3)) * 2560 + 2556, 0);
        if (__builtin_amdgcn_ballot_w64(f == SENT) == 0) break;
        if (--budget < 0) break;
        __builtin_amdgcn_s_sleep(1);
        asm volatile("" ::: "memory");
      }
      asm volatile("" ::: "memory");
      u32x4 px[4][2], pm[2], wo[2];
      for (;;) {
        unsigned mx = 0;
#pragma unroll
        for (int hh = 0; hh < 4; ++hh)
#pragma unroll
          for (int tt = 0; tt < 2; ++tt) {
            const unsigned po = pt_off(s) + (4 * wave + hh) * 2560;
            px[hh][tt] = ld16(rs, tt * 1024 + lane * 16, po);
            pm[tt][hh] = ld4(rs, 2048 + tt * 256 + lane * 4, po);
          }
#pragma unroll
        for (int nt = 0; nt < 2; ++nt)
          wo[nt] = ld16(rs, lane * 16, (s < PT_ARM ? OFF_WT + s * 28 * 4096 : OFF_WTA + (s - PT_ARM) * 28 * 4096) + ((2 * j + nt) * 4 + wave) * 1024);
#pragma unroll
        for (int hh = 0; hh < 4; ++hh)
#pragma unroll
          for (int tt = 0; tt < 2; ++tt) mx = umax4(mx, px[hh][tt]);
        mx = umax4(umax4(umax4(umax4(mx, pm[0]), pm[1]), wo[0]), wo[1]);
        if (__builtin_amdgcn_ballot_w64(mx == SENT) == 0) break;
        if (--budget < 0) break;
        __builtin_amdgcn_s_sleep(SL_TOK);
        asm volatile("" ::: "memory");
      }
#pragma unroll
      for (int tt = 0; tt < 2; ++tt)
#pragma unroll
        for (int nt = 0; nt < 2; ++nt) {
          f32x4 d = {0, 0, 0, 0};
#pragma unroll
          for (int hh = 0; hh < 4; ++hh) d = mf32(asb8(px[hh][tt]), asb8(vo[2 * hh + nt]), d);
          d = mf32(asb8(pm[tt]), asb8(wo[nt]), d);
#pragma unroll
          for (int r = 0; r < 4; ++r) *(float*)(RED + ((wave * 32 + 16 * tt + 4 * g + r) * 32 + 16 * nt + c) * 4) = d[r];
        }
      __syncthreads();
      f32x4 av = *(const f32x4*)(RED + tok * 128 + q8 * 16);
#pragma unroll
      for (int ww = 1; ww < 4; ++ww) av += *(const f32x4*)(RED + (ww * 32 + tok) * 128 + q8 * 16);
      const float og = rbf(1.f - gt);
      float o[4];
#pragma unroll
      for (int i = 0; i < 4; ++i) o[i] = rbf(gt * rbf(av[i])) + rbf(og * bv[i]);
      *(u32x2*)(out + (s * 32 + tok) * DM + j * 32 + q8 * 4) = (u32x2){pk(o[0], o[1]), pk(o[2], o[3])};
      __syncthreads();
    }
    return;
  }

  const bool isA = role < 16;
  const int ntile = isA ? 7 : 8;
  const unsigned wbase = isA ? OFF_WA + (role * 4 + wave) * 7 * TILE_B : OFF_WB + ((role - 16) * 4 + wave) * 8 * TILE_B;
  const char* wsrc = ws + wbase;
  if (team >= 0) {
    for (int i = isA ? STAG_A + team : STAG_B * team; i > 0; --i) __builtin_amdgcn_s_sleep(12);
  }
  char* wl = smem + wave * (ntile - 6) * TILE_B;
  if (team >= 0 && isA) {
    const char* src = wsrc + 6 * TILE_B + fresh_lane() * 16;
    for (int i = 0; i < (ntile - 6) * 14; ++i)
      __builtin_amdgcn_global_load_lds((const GAS void*)(src + i * 1024), (LAS void*)(wl + i * 1024), 16, 0, 0);
  }
#define LOAD_W() bf16x16 w[6][7]; \
  _Pragma("unroll") for (int t = 0; t < 6; ++t) \
  _Pragma("unroll") for (int ks = 0; ks < 7; ++ks) \
    w[t][ks] = cat16(__builtin_amdgcn_raw_buffer_load_b128(rs, lane * 16, wbase + t * TILE_B + ks * 2048, 0), \
                     __builtin_amdgcn_raw_buffer_load_b128(rs, lane * 16 + 1024, wbase + t * TILE_B + ks * 2048, 0))
#define LANE_VARS() const int lane = fresh_lane(), tid = wave * 64 + lane; (void)tid; \
  const int c = lane & 15, g = lane >> 4; \
  const int spidx = (lane & 2) ? 0xEEEE : 0x4444; \
  (void)c; (void)g; (void)spidx
  const f32x4 Z = {0, 0, 0, 0};

  if (team < 0 && isA) {
    LANE_VARS();
    const int h = role;
    constexpr unsigned TLB = 12288, SX = 32768, TS = 40;
    const unsigned MS = wave * 3072, TL = TLB + wave * 64 * TS;
    const int p0 = wave == 0 ? 0 : 2 * wave - 1;
    if (lane < 40) *(u32x2*)(smem + TL + 56 * TS + lane * 8) = (u32x2){0, 0};
#pragma unroll
    for (int i = 0; i < 4; ++i)
      *(u32x4*)(smem + SX + (i >> 1) * 16384 + wave * 3072 + (i & 1) * 1024 + lane * 16) = (u32x4){SENT, SENT, SENT, SENT};
    unsigned sxw = SX + g * 3072 + c * 16 + ((wave + (c >> 3) + 2 * g) & 3) * 4, smr = SX + wave * 3072 + 1024 + (g & 1) * 256 + c * 16;
    unsigned pin = (lane & 1) ? (h * 4 + wave) * PM_SLOT + (c >> 2) * 16 + (g & 1) * 8 + ((c >> 1) & 1) * 4
                              : (h & 7) * 512 + wave * 128 + (g >> 1) * 64 + ((c >> 1) & 1) * 32 + (g & 1) * 16 + (c >> 2) * 4;
    asm volatile(SCORE_ASM
                 : "+v"(pin), "+v"(sxw), "+v"(smr)
                 : "v"(lane * 16), "v"(MS + lane * 16),
                   "v"(MS + (7 * wave - 4 * p0 + (g >> 1)) * 256 + ((c >> 1) & 3) * 64 + (g & 1) * 32 + (c & 1) * 16),
                   "v"(TL + c * TS + g * 8), "v"(TL + (48 + 2 * (c >> 2) + (c & 1)) * TS + g * 8 + ((c >> 1) & 1) * 4),
                   "v"(TL + (g * 8 + (c >> 2)) * TS + (c & 3) * 8),
                   "v"(spidx),
                   "s"(rs), "s"(wbase), "s"(p0), "s"(h >> 3), "s"(OFF_XK + wave * 8192), "s"(OFF_MEM0),
                   "s"(__builtin_amdgcn_readfirstlane(budget)), "s"(OFF_PROF + (role * 4 + wave) * 4096),
                   "i"(OFF_CFG), "s"(OFF_RDY + h * 64)
                 : SCORE_CLOB);
    __builtin_unreachable();
  }

  if (team < 0) {
    LANE_VARS();
    const int j = role - 16, nt = bi & 1;
    unsigned m0a = OFF_MEM0 + wave * 2048, m0b = m0a + (wave < 3 ? 1024 : 0), vbo = OFF_VB + j * 32768 + nt * 1024;
    unsigned vfo = OFF_VF + bi * 64, mto = OFF_MEMT1 - MEM_STRIDE + bi * 1024, wto = OFF_WT + (bi * 4 + wave) * 1024, wfo = OFF_WF + bi * 64;
    asm volatile(VALUE_ASM
                 : "+s"(m0a), "+s"(m0b), "+s"(vbo), "+s"(vfo), "+s"(mto), "+s"(wto), "+s"(wfo)
                 : "v"(lane * 16), "v"(wave * 2048 + lane * 16),
                   "v"((g >> 1) * 512 + (c >> 2) * 128 + (g & 1) * 64 + (c & 3) * 16),
                   "v"(g * 64 + (c >> 3) * 32 + (c & 1) * 16 + ((c & 7) >> 1) * 4), "v"(spidx),
                   "v"((lane & 1) ? 0x03020706 : 0x05040100),
                   "s"(rs), "s"(wbase + nt * TILE_B), "s"(wave), "s"(bi >> 4), "s"((bi & 15) * 256),
                   "s"(__builtin_amdgcn_readfirstlane(budget)), "s"(OFF_PROF + ((16 + (bi >> 1)) * 4 + nt) * 4096),
                   "s"(__builtin_amdgcn_readfirstlane((bi < 7 && wave == 2) ? 1 : 0)), "s"(OFF_MEMT1 + 62 * MEM_STRIDE + bi * 256),
                   "s"(OFF_WTA + (bi * 4 + wave) * 1024), "i"(OFF_CFG)
                 : VALUE_CLOB);
    __builtin_unreachable();
  }

  if (isA) {
    LANE_VARS();
    LOAD_W();
    const int h = role;
    char* XP = smem + 57344;
    char* SXX = XP + 14 * XP_SK;
    char* SMM = SXX + 4 * 32 * 144;
    stage_x<false>(x, team, XP, tid);
    __builtin_amdgcn_s_waitcnt(0x0F70);
    for (int s = team; s < NSEG; s += NTA) {
      LANE_VARS();
      const int tok = tid >> 3, q8 = tid & 7;
      wg_barrier();
      u32x4 va[4], vb[4], kx[2][3];
      u32x2 kx6[2];
#pragma unroll
      for (int kt = 0; kt < 2; ++kt) {
        const unsigned xo = OFF_XK + (s * 4 + wave) * 8192 + kt * 4096;
#pragma unroll
        for (int p = 0; p < 3; ++p) kx[kt][p] = __builtin_amdgcn_raw_buffer_load_b128(rs, lane * 16 + p * 1024, xo, 0);
        kx6[kt] = __builtin_amdgcn_raw_buffer_load_b64(rs, 3072 + ((g >> 1) * 16 + c) * 16 + (g & 1) * 8, xo, 0);
      }
      fetch_x(x, min(s + NTA, NSEG - 1), tid, va, vb);
      f32x4 acc[7][2];
#pragma unroll
      for (int t = 0; t < 7; ++t) { acc[t][0] = Z; acc[t][1] = Z; }
      {
        const char* xr = XP + (lane & 31) * 16 + (lane >> 5) * XP_SG;
        const char* wr = wl + lane * 16;
        bf16x8 pf[2][3];
        pf[0][0] = *(const bf16x8*)xr; pf[0][1] = *(const bf16x8*)(xr + XP_ST); pf[0][2] = *(const bf16x8*)wr;
#pragma unroll
        for (int k2 = 0; k2 < 14; ++k2) {
          if (k2 + 1 < 14) {
            pf[(k2 + 1) & 1][0] = *(const bf16x8*)(xr + (k2 + 1) * XP_SK);
            pf[(k2 + 1) & 1][1] = *(const bf16x8*)(xr + (k2 + 1) * XP_SK + XP_ST);
            pf[(k2 + 1) & 1][2] = *(const bf16x8*)(wr + (k2 + 1) * 1024);
          }
          __builtin_amdgcn_sched_barrier(0);
#pragma unroll
          for (int p = 0; p < 7; ++p) {
            const bf16x8 wa = p < 6 ? half8(w[p < 6 ? p : 0][k2 >> 1], k2 & 1) : pf[k2 & 1][2];
            const int t = p == 5 ? 6 : (p == 6 ? 5 : p);
            acc[t][0] = mf32(wa, pf[k2 & 1][0], acc[t][0]);
            acc[t][1] = mf32(wa, pf[k2 & 1][1], acc[t][1]);
          }
          __builtin_amdgcn_sched_barrier(0);
        }
      }
      u32x2 tb[7][2];
#pragma unroll
      for (int t = 0; t < 7; ++t)
#pragma unroll
        for (int tt = 0; tt < 2; ++tt) tb[t][tt] = (u32x2){pk(acc[t][tt][0], acc[t][tt][1]), pk(acc[t][tt][2], acc[t][tt][3])};
#pragma unroll
      for (int kt = 0; kt < 2; ++kt) {
#pragma unroll
        for (int tt = kt; tt < 2; ++tt) {
          f32x4 sa = Z;
#pragma unroll
          for (int p = 0; p < 3; ++p)
            sa = mf32(asb8(kx[kt][p]), asb8((u32x4){tb[2 * p][tt][0], tb[2 * p + 1][tt][0], tb[2 * p][tt][1], tb[2 * p + 1][tt][1]}), sa);
          sa = mf16(kx6[kt], tb[6][tt], sa);
          *(f32x4*)(SXX + (wave * 32 + 16 * tt + c) * 144 + (16 * kt + 4 * g) * 4) = sa;
        }
      }
      asm volatile("s_waitcnt vmcnt(0) lgkmcnt(0)\n\ts_barrier" ::: "memory");
      if (tid == 0 && s != team) st4(rs, 0, OFF_PTF + ((s - NTA) * 16 + h) * 64, 1u);
      f32x4 sx = *(const f32x4*)(SXX + tok * 144 + q8 * 16);
#pragma unroll
      for (int ww = 1; ww < 4; ++ww) sx += *(const f32x4*)(SXX + (ww * 32 + tok) * 144 + q8 * 16);
#pragma unroll
      for (int i = 0; i < 4; ++i) sx[i] = (4 * q8 + i <= tok) ? sx[i] : -INFINITY;

      put_x<false>(XP, tid, va, vb);
      if (s == NSEG - 1) wait_final(rs, FIN_A, budget);
      u32x4 ka[3]; u32x2 k6;
      for (;;) {
        unsigned mx = 0;
#pragma unroll
        for (int p = 0; p < 3; ++p) {
          const int col = 112 * wave + 32 * p + 8 * g;
          ka[p] = ld16(rs, (col >> 4) * 256 + ((c >> 1) & 3) * 64 + ((col >> 3) & 1) * 32 + (c & 1) * 16 + (col & 7) * 2, memt_off(s));
        }
        { const int col = 112 * wave + 96 + 4 * g; k6 = ld8(rs, (col >> 4) * 256 + ((c >> 1) & 3) * 64 + ((col >> 3) & 1) * 32 + (c & 1) * 16 + (col & 7) * 2, memt_off(s)); }
#pragma unroll
        for (int p = 0; p < 3; ++p) mx = umax4(mx, ka[p]);
        mx = umax3(mx, k6[0], k6[1]);
        if (__builtin_amdgcn_ballot_w64(mx == SENT) == 0) break;
        if (--budget < 0) break;
        __builtin_amdgcn_s_sleep(SL_TOK);
        asm volatile("" ::: "memory");
      }
#pragma unroll
      for (int tt = 0; tt < 2; ++tt) {
        f32x4 sa = Z;
#pragma unroll
        for (int p = 0; p < 3; ++p)
          sa = mf32(asb8(ka[p]), asb8((u32x4){tb[2 * p][tt][0], tb[2 * p + 1][tt][0], tb[2 * p][tt][1], tb[2 * p + 1][tt][1]}), sa);
        sa = mf16(k6, tb[6][tt], sa);
        if (g < 2) *(f32x4*)(SMM + (wave * 32 + 16 * tt + c) * 32 + g * 16) = sa;
      }
      __syncthreads();
      float sv = 0.f;
#pragma unroll
      for (int ww = 0; ww < 4; ++ww) sv += *(const float*)(SMM + (ww * 32 + tok) * 32 + q8 * 4);
      float mx = fmaxf(fmaxf(fmaxf(sx[0], sx[1]), fmaxf(sx[2], sx[3])), sv);
      mx = fmaxf(mx, dpp<0xB1>(mx)); mx = fmaxf(mx, dpp<0x4E>(mx)); mx = fmaxf(mx, dpp<0x141>(mx));
      float e[4];
#pragma unroll
      for (int i = 0; i < 4; ++i) e[i] = __builtin_amdgcn_exp2f(sx[i] - mx);
      const float em = __builtin_amdgcn_exp2f(sv - mx);
      float sum = (e[0] + e[1]) + (e[2] + e[3]) + em;
      sum += dpp<0xB1>(sum); sum += dpp<0x4E>(sum); sum += dpp<0x141>(sum);
      const float inv = __builtin_amdgcn_rcpf(sum);
      const unsigned po = pt_off(s) + h * 2560;
      st8(rs, (tok >> 4) * 1024 + (q8 >> 1) * 256 + (tok & 15) * 16 + (q8 & 1) * 8, po, (u32x2){canon(pk(e[0] * inv, e[1] * inv)), canon(pk(e[2] * inv, e[3] * inv))});
      const float pmv = em * inv;
      const unsigned dw = canon(pk(pmv, dpp<0xB1>(pmv)));
      if (!(q8 & 1)) st4(rs, 2048 + (tok >> 4) * 256 + (q8 >> 1) * 64 + (tok & 15) * 4, po, dw);
      if (s + NTA >= NSEG) {
        asm volatile("s_waitcnt vmcnt(0)\n\ts_barrier" ::: "memory");
        if (tid == 0) st4(rs, 0, OFF_PTF + (s * 16 + h) * 64, 1u);
      }
    }
    return;
  }

  {
    const int j = jj >> 1, nt = jj & 1;
    bf16x16 w[4][7];
    {
      LANE_VARS();
      u32x4 va[4], vb[4];
      fetch_x(x, team, tid, va, vb);
#pragma unroll
      for (int t = 0; t < 4; ++t)
#pragma unroll
        for (int ks = 0; ks < 7; ++ks)
          w[t][ks] = cat16(__builtin_amdgcn_raw_buffer_load_b128(rs, lane * 16, wbase + (2 * t + nt) * TILE_B + ks * 2048, 0),
                           __builtin_amdgcn_raw_buffer_load_b128(rs, lane * 16 + 1024, wbase + (2 * t + nt) * TILE_B + ks * 2048, 0));
      put_x<true>(smem, tid, va, vb);
    }
    __builtin_amdgcn_s_waitcnt(0x0F70);
    wg_barrier();
    int buf = 0;
    for (int s = team; s < NSEG; s += NTB) {
      LANE_VARS();
      u32x4 va[4], vb[4];
      fetch_x(x, min(s + NTB, NSEG - 1), tid, va, vb);
      f32x4 acc[4][2];
#pragma unroll
      for (int t = 0; t < 4; ++t) { acc[t][0] = Z; acc[t][1] = Z; }
      const char* xr = smem + buf + (lane & 31) * 16 + (lane >> 5) * XP_SG;
      bf16x8 pf[3][2];
#pragma unroll
      for (int k2 = 0; k2 < 2; ++k2) { pf[k2][0] = *(const bf16x8*)(xr + k2 * XP_SK); pf[k2][1] = *(const bf16x8*)(xr + k2 * XP_SK + XP_ST); }
#pragma unroll
      for (int k2 = 0; k2 < 14; ++k2) {
        if (k2 + 2 < 14) {
          pf[(k2 + 2) % 3][0] = *(const bf16x8*)(xr + (k2 + 2) * XP_SK);
          pf[(k2 + 2) % 3][1] = *(const bf16x8*)(xr + (k2 + 2) * XP_SK + XP_ST);
        }
        __builtin_amdgcn_sched_barrier(0);
#pragma unroll
        for (int t = 0; t < 4; ++t) {
          const bf16x8 wb = half8(w[t][k2 >> 1], k2 & 1);
          acc[t][0] = mf32(pf[k2 % 3][0], wb, acc[t][0]);
          acc[t][1] = mf32(pf[k2 % 3][1], wb, acc[t][1]);
        }
        __builtin_amdgcn_sched_barrier(0);
      }
#pragma unroll
      for (int t = 0; t < 4; ++t)
        st16(rs, lane * 16 + (2 * t + nt) * 1024, OFF_VB + ((s * 14 + j) * 4 + wave) * 8192,
             (u32x4){pk(acc[t][0][0], acc[t][0][1]), pk(acc[t][0][2], acc[t][0][3]), pk(acc[t][1][0], acc[t][1][1]), pk(acc[t][1][2], acc[t][1][3])});
      asm volatile("" ::: "memory");
      buf ^= 32768;
      put_x<true>(smem + buf, tid, va, vb);
      const bool imm = s < team + V_IMM * NTB || s + NTB >= NSEG, prev = s >= team + (V_IMM + 1) * NTB;
      if (imm) asm volatile("s_waitcnt vmcnt(0)" ::: "memory");
      else asm volatile("s_waitcnt vmcnt(4)" ::: "memory");
      wg_barrier();
      if (tid == 0) {
        if (prev) st4(rs, 0, OFF_VF + ((s - NTB) * 28 + jj) * 64, 1u);
        if (imm) st4(rs, 0, OFF_VF + (s * 28 + jj) * 64, 1u);
      }
    }
  }
}

extern "C" void kernel_launch(void* const* d_in, const int* in_sizes, int n_in,
                              void* d_out, int out_size, void* d_ws, size_t ws_size,
                              hipStream_t stream) {
  const u16* x = (const u16*)d_in[0];
  const u16* mem0 = (const u16*)d_in[1];
  const u16* Wq = (const u16*)d_in[2];
  const u16* Wk = (const u16*)d_in[3];
  const u16* Wv = (const u16*)d_in[4];
  const u16* Wo = (const u16*)d_in[5];
  const u16* w_r = (const u16*)d_in[8];
  const u16* w_gate = (const u16*)d_in[9];
  const u16* w_up = (const u16*)d_in[10];
  const u16* w_down = (const u16*)d_in[11];
  const u16* gate_w = (const u16*)d_in[12];
  (void)hipFuncSetAttribute((const void*)prep_kernel, hipFuncAttributeMaxDynamicSharedMemorySize, K2_LDS);
  (void)hipFuncSetAttribute((const void*)main_kernel, hipFuncAttributeMaxDynamicSharedMemorySize, K2_LDS);
  prep_kernel<<<256, 512, K2_LDS, stream>>>(x, mem0, Wq, Wk, Wv, Wo, w_r, gate_w, (char*)d_ws);
  main_kernel<<<256, 256, K2_LDS, stream>>>(x, w_gate, w_up, w_down, (u16*)d_out, (char*)d_ws
                                            );
}
```
